```python
import torch

def _softmax(x, axis=-1):
    return torch.softmax(x, dim=axis)
def _gelu(x):
    return torch.nn.functional.gelu(x, approximate="tanh")
def _silu(x):
    return torch.nn.functional.silu(x)
def _sigmoid(x):
    return torch.sigmoid(x)
def _layernorm(x, axis=-1, eps=1e-5):
    m = x.mean(dim=axis, keepdim=True)
    v = x.var(dim=axis, keepdim=True, unbiased=False)
    return (x - m) / torch.sqrt(v + eps)
def _scatter_add(tgt, idx, src, axis=0):
    return tgt.clone().index_add_(axis, idx, src)
def _segment_sum(src, seg, n):
    out = torch.zeros((n,) + src.shape[1:], dtype=src.dtype)
    return out.index_add_(0, seg, src)
def _pad(x, pw):
    flat = tuple(v for pair in reversed(pw) for v in pair)
    return torch.nn.functional.pad(x, flat)

kernel_name = 'parallel_sum__mha_block__dense_activation_relu__prompt'

def setup_inputs():

    _w = (lambda a: (lambda t: t.to(torch.bfloat16) if t.is_floating_point() else t)(torch.as_tensor(a)))
    def _A_setup():
        B, S, D, H = 1, 2048, 1024, 32
        return {
            "x": _w((lambda *s: torch.randn(*s, dtype=torch.bfloat16) * 0.05)(B, S, D)),
            "wqkv": _w((lambda *s: torch.randn(*s, dtype=torch.bfloat16) * 0.05)(D, 3 * D)),
            "wo": _w((lambda *s: torch.randn(*s, dtype=torch.bfloat16) * 0.05)(D, D)),
        }
    _A_in = _A_setup()

    def _B_setup():
        B, S, D, Dff = 1, 2048, 1024, 3584
        return {
            "x": _w((lambda *s: torch.randn(*s, dtype=torch.bfloat16) * 0.05)(B, S, D)),
            "w1": _w((lambda *s: torch.randn(*s, dtype=torch.bfloat16) * 0.05)(D, Dff)),
            "w2": _w((lambda *s: torch.randn(*s, dtype=torch.bfloat16) * 0.05)(Dff, D)),
        }
    _B_in = _B_setup()

    return {
        "A_x": _A_in["x"],
        "A_wqkv": _A_in["wqkv"],
        "A_wo": _A_in["wo"],
        "B_x": _B_in["x"],
        "B_w1": _B_in["w1"],
        "B_w2": _B_in["w2"],
    }

def reference(A_x, A_wqkv, A_wo, B_x, B_w1, B_w2):
    def _A_ref(x, wqkv, wo):
        B, S, D = x.shape
        H = 32
        Dh = D // H
        qkv = (lambda x, *d: x.permute(*d))((x @ wqkv).reshape(B, S, 3, H, Dh), 2, 0, 3, 1, 4)
        q, k, v = qkv[0], qkv[1], qkv[2]
        scores = torch.einsum("bhqd,bhkd->bhqk", q, k) * (Dh ** -0.5)
        p = _softmax(scores, axis=-1)
        o = torch.einsum("bhqk,bhkd->bhqd", p, v)
        return (lambda x, *d: x.permute(*d))(o, 0, 2, 1, 3).reshape(B, S, D) @ wo

    def _B_ref(x, w1, w2):
        h = torch.einsum("bsd,df->bsf", x, w1)
        zero = torch.zeros_like(h)
        h = torch.maximum(h, zero)
        return torch.einsum("bsf,fd->bsd", h, w2)

    x = A_x
    xn = _layernorm(x, axis=-1)
    return x + _A_ref(xn, A_wqkv, A_wo) + _B_ref(xn, B_w1, B_w2)
```

```cpp
#include <hip/hip_runtime.h>
#include <hip/hip_bf16.h>
#include <stdint.h>

typedef unsigned short bf16_t;
typedef short bf16x8 __attribute__((ext_vector_type(8)));
typedef short s16x4 __attribute__((ext_vector_type(4)));
typedef float f32x2 __attribute__((ext_vector_type(2)));
typedef float f32x4 __attribute__((ext_vector_type(4)));
typedef float f32x16 __attribute__((ext_vector_type(16)));
typedef unsigned u32x4 __attribute__((ext_vector_type(4)));
typedef unsigned u32x2 __attribute__((ext_vector_type(2)));
typedef __bf16 bf16x2n __attribute__((ext_vector_type(2)));
#define LAS __attribute__((address_space(3)))
typedef LAS char* lds_ptr;

constexpr int S_ = 2048, D_ = 1024, H_ = 32, DH = 32, DFF = 3584;
constexpr int LDAO = DFF + D_;
constexpr size_t WS_FLAGS = 0;
constexpr size_t WS_XN = 1u << 20;
constexpr size_t WS_QKV = WS_XN + (size_t)S_ * D_ * 2;
constexpr size_t WS_AO = WS_QKV + (size_t)3 * S_ * D_ * 2;
constexpr size_t WS_PART = WS_AO + (size_t)S_ * LDAO * 2;
constexpr int LDS_BYTES = 151552;
constexpr int F_Q = 0, F_H = 16, F_O = 24, F_P = 32;

#define EXP_NOHST 0
#define STAMP(k, i) do {} while (0)

__device__ __forceinline__ unsigned cvtpk(float lo, float hi) { return __builtin_bit_cast(unsigned, __builtin_convertvector((f32x2){lo, hi}, bf16x2n)); }
__device__ __forceinline__ float rbf(float x) { return __uint_as_float(cvtpk(x, x) << 16); }
typedef __amdgpu_buffer_rsrc_t rsrc_t;
__device__ __forceinline__ rsrc_t mkrsrc(const void* p, unsigned bytes) { return __builtin_amdgcn_make_buffer_rsrc((void*)p, 0, bytes, 0x00020000); }
__device__ __forceinline__ void bl_lds(rsrc_t r, lds_ptr l, unsigned voff, unsigned soff) {
    asm volatile("s_mov_b32 m0, %0\n\ts_nop 0\n\tbuffer_load_dwordx4 %1, %2, %3 offen lds" :: "s"(__builtin_amdgcn_readfirstlane((unsigned)(uintptr_t)l)), "v"(voff), "s"(r), "s"(__builtin_amdgcn_readfirstlane(soff)) : "memory"); }
__device__ __forceinline__ void bl_lds_dev(rsrc_t r, lds_ptr l, unsigned voff, unsigned soff) {
    asm volatile("s_mov_b32 m0, %0\n\ts_nop 0\n\tbuffer_load_dwordx4 %1, %2, %3 offen sc1 lds" :: "s"(__builtin_amdgcn_readfirstlane((unsigned)(uintptr_t)l)), "v"(voff), "s"(r), "s"(__builtin_amdgcn_readfirstlane(soff)) : "memory"); }
__device__ __forceinline__ void st_wt(u32x4 v, rsrc_t r, unsigned off) { __builtin_amdgcn_raw_buffer_store_b128(v, r, off, 0, 16); }
__device__ __forceinline__ bf16x8 lds_b128(lds_ptr p) { return *(const LAS bf16x8*)p; }
__device__ __forceinline__ s16x4 lds_tr(lds_ptr p) { return __builtin_amdgcn_ds_read_tr16_b64_v4i16((LAS s16x4*)p); }
__device__ __forceinline__ bf16x8 cat4(s16x4 a, s16x4 b) { return (bf16x8){a[0], a[1], a[2], a[3], b[0], b[1], b[2], b[3]}; }
#define MFMA16(a, b, c) __builtin_amdgcn_mfma_f32_16x16x32_bf16(a, b, c, 0, 0, 0)
#define MFMA32(a, b, c) __builtin_amdgcn_mfma_f32_32x32x16_bf16(a, b, c, 0, 0, 0)
#define WAIT_BAR(N) asm volatile("s_waitcnt vmcnt(%0) lgkmcnt(0)\n\ts_barrier" :: "n"(N) : "memory")
#define LGKM_BAR() asm volatile("s_waitcnt lgkmcnt(0)\n\ts_barrier" ::: "memory")
#define SB0() __builtin_amdgcn_sched_barrier(0)
#define RLX_AGENT __ATOMIC_RELAXED, __HIP_MEMORY_SCOPE_AGENT
template <int I> struct IC { static constexpr int value = I; };
template <int B, int E, class F> __device__ __forceinline__ void unroll(F&& f) { if constexpr (B < E) { f(IC<B>{}); unroll<B + 1, E>(f); } }

__device__ __forceinline__ void poll_acquire(unsigned* flags, int line, unsigned target, int n) {
    const bool act = (int)(threadIdx.x & 63) < n; unsigned spins = 0;
    for (;;) { unsigned v = target; if (act) v = __hip_atomic_load(flags + line * 32, RLX_AGENT);
        if (__all(v >= target)) break; __builtin_amdgcn_s_sleep(2); if (++spins > (1u << 22)) break; }
    asm volatile("" ::: "memory");
}
__device__ __forceinline__ void publish(unsigned* flags, int line) {
    if (threadIdx.x == 0) __hip_atomic_fetch_add(flags + line * 32, 1u, RLX_AGENT);
}

__global__ __launch_bounds__(512) void ln_kernel(const bf16_t* __restrict__ x, bf16_t* __restrict__ xn, unsigned* __restrict__ flags) {
    const int tid = threadIdx.x, lane = tid & 63, row = blockIdx.x * 8 + (tid >> 6);
    if (blockIdx.x < 32) flags[blockIdx.x * 512 + tid] = 0u;
    const u32x4* src = (const u32x4*)(x + (size_t)row * D_);
    u32x4 v0 = src[lane], v1 = src[64 + lane];
    float f[16];
#pragma unroll
    for (int i = 0; i < 4; ++i) { f[2 * i] = __uint_as_float(v0[i] << 16); f[2 * i + 1] = __uint_as_float(v0[i] & 0xffff0000u); f[8 + 2 * i] = __uint_as_float(v1[i] << 16); f[8 + 2 * i + 1] = __uint_as_float(v1[i] & 0xffff0000u); }
    float s = 0.f;
#pragma unroll
    for (int i = 0; i < 16; ++i) s += f[i];
#pragma unroll
    for (int o = 32; o >= 1; o >>= 1) s += __shfl_xor(s, o);
    const float mean = s * (1.0f / D_);
    float q = 0.f;
#pragma unroll
    for (int i = 0; i < 16; ++i) { const float d = f[i] - mean; q += d * d; }
#pragma unroll
    for (int o = 32; o >= 1; o >>= 1) q += __shfl_xor(q, o);
    const float mb = rbf(mean), vb = rbf(q * (1.0f / D_));
    const float sd = rbf(__fsqrt_rn(rbf(vb + 1e-5f)));
    u32x4 o0, o1;
#pragma unroll
    for (int i = 0; i < 4; ++i) {
        o0[i] = cvtpk(__fdiv_rn(rbf(f[2 * i] - mb), sd), __fdiv_rn(rbf(f[2 * i + 1] - mb), sd));
        o1[i] = cvtpk(__fdiv_rn(rbf(f[8 + 2 * i] - mb), sd), __fdiv_rn(rbf(f[8 + 2 * i + 1] - mb), sd));
    }
    u32x4* dst = (u32x4*)(xn + (size_t)row * D_);
    dst[lane] = o0; dst[64 + lane] = o1;
}

constexpr int HSTG = 65536;
template <int NT>
__device__ __forceinline__ void gemm1_body(lds_ptr lds, const bf16_t* xn, const bf16_t* W, const int ldw, const int m0, const int n0, bf16_t* qkv, bf16_t* ao) {
    constexpr int BN = 32 * NT, RB = BN * 2, CPR = BN / 8, BSLOT = 64 * RB, NKT = D_ / 64, GB = (NT == 7) ? 4 : 3;
    constexpr int LB = 65536, NMF = 4 * NT, NRD = 4 + 2 * NT, NBB = (NT == 7) ? 2 : 4;
    const int tid = threadIdx.x, lane = tid & 63, wid = __builtin_amdgcn_readfirstlane(tid >> 6), wm = wid & 3, wn = wid >> 2;
    const int fr = lane & 15, g = lane >> 4, q = fr >> 2, p = lane & 3;
    const rsrc_t rA = mkrsrc(xn + (size_t)m0 * D_, 256 * D_ * 2), rB = mkrsrc(W + n0, (unsigned)(D_ * ldw - n0) * 2u);
    const unsigned aoff = (unsigned)((32 * wid + (lane >> 3)) * D_ + (((lane & 7) ^ ((lane >> 3) & 7)) * 8)) * 2u;
    unsigned boff[GB];
#pragma unroll
    for (int i = 0; i < GB; ++i) { const int idx = 64 * i + lane, rl = idx / CPR, cp = idx % CPR, k = (8 * wid + rl) & 63;
        const int swz = (NT == 7) ? 2 * ((k >> 3) & 1) : 2 * (((k >> 1) & 1) | (((k >> 3) & 1) << 1));
        boff[i] = (unsigned)(k * ldw + (cp ^ swz) * 8) * 2u; }
    auto stageA1 = [&](int kt, int slot, int i) { bl_lds(rA, lds + slot * 32768 + (32 * wid + 8 * i) * 128, aoff, kt * 128 + i * (8 * D_ * 2)); };
    auto stageB1 = [&](int kt, int slot, int i) { const unsigned so = (unsigned)(kt * 64 * ldw) * 2u;
        if (NT == 7 && i == 3) { if (lane < 32) bl_lds(rB, lds + LB + slot * BSLOT + (8 * wid * CPR + 192) * 16, boff[GB - 1], so); }
        else bl_lds(rB, lds + LB + slot * BSLOT + (8 * wid * CPR + 64 * i) * 16, boff[i < GB ? i : 0], so); };
    int ar[2];
#pragma unroll
    for (int ks = 0; ks < 2; ++ks) ar[ks] = (64 * wm + fr) * 128 + (((4 * ks + g) ^ (fr & 7)) * 16);
    int br[NBB];
    { const int swz = (NT == 7) ? 2 * (g & 1) : 2 * ((q >> 1) | ((g & 1) << 1));
#pragma unroll
      for (int j = 0; j < NBB; ++j) br[j] = LB + (8 * g + q) * RB + (((2 * (NT * wn + j) + (p >> 1)) ^ swz) * 16) + (p & 1) * 8; }
    f32x4 acc[4][NT];
#pragma unroll
    for (int i = 0; i < 4; ++i)
#pragma unroll
        for (int j = 0; j < NT; ++j) acc[i][j] = (f32x4){0.f, 0.f, 0.f, 0.f};
    bf16x8 A0[4], A1[4]; s16x4 B0[NT][2], B1[NT][2];
#define RDONE(AN, BN_, sa, sb, ks, r) do { if ((r) < 4) AN[(r) < 4 ? (r) : 0] = lds_b128(lds + (sa) + ar[ks] + ((r) < 4 ? (r) : 0) * 2048); \
        else if ((r) < NRD) { constexpr int jj = (((r) - 4) >> 1) < NT && (r) >= 4 ? (((r) - 4) >> 1) : 0, tt = ((r) - 4) & 1; BN_[jj][tt] = lds_tr(lds + (sb) + br[jj % NBB] + 32 * (jj - jj % NBB) + (32 * (ks) + 4 * tt) * RB); } } while (0)
#define SUBSTEP(AC, BC, AN, BN_, sa, sb, ks, STG, go, tl, sl) do { \
        unroll<0, NMF>([&](auto ic) { constexpr int idx = decltype(ic)::value; constexpr int j = idx >> 2, i = idx & 3; \
            acc[i][j] = MFMA16(cat4(BC[j][0], BC[j][1]), AC[i], acc[i][j]); SB0(); \
            RDONE(AN, BN_, sa, sb, ks, idx); \
            if ((idx % NT) == 1 && (go)) { if (STG == 1) stageA1(tl, sl, idx / NT); if (STG == 2 && idx / NT < GB) stageB1(tl, sl, idx / NT); } \
            SB0(); }); } while (0)
    STAMP(1, 0);
#pragma unroll
    for (int t = 0; t < 2; ++t) {
#pragma unroll
        for (int i = 0; i < GB; ++i) stageB1(t, t, i);
#pragma unroll
        for (int i = 0; i < 4; ++i) stageA1(t, t, i); }
    WAIT_BAR(4 + GB);
    STAMP(1, 1);
    unroll<0, NRD>([&](auto ic) { constexpr int idx = decltype(ic)::value; RDONE(A0, B0, 0, 0, 0, idx); });
    int sa = 0, sb = 0, sb3 = 0;
    for (int t = 0; t < NKT; ++t) {
        const int tn = min(t + 2, NKT - 1); constexpr bool go = true;
        SB0();
        { const int slb = (sb3 == 0) ? 2 : sb3 - 1;
          SUBSTEP(A0, B0, A1, B1, sa, sb, 1, 2, go, tn, slb); }
        WAIT_BAR(GB);
        SB0();
        sa ^= 32768; sb3 = (sb3 == 2) ? 0 : sb3 + 1; sb = sb3 * BSLOT;
        SUBSTEP(A1, B1, A0, B0, sa, sb, 0, 1, go, tn, t & 1);
    }
#undef RDONE
#undef SUBSTEP
    STAMP(1, 2);
    const int mrow = m0 + 64 * wm + fr;
    if constexpr (NT == 6) {
        const rsrc_t rsq = mkrsrc(qkv, 3 * S_ * D_ * 2);
#pragma unroll
        for (int b = 0; b < 3; ++b) {
            const int col = n0 + 96 * wn + 32 * b, which = col >> 10, head = (col & 1023) >> 5;
            const float sc = (which == 0) ? 0.17677669529663687f * 1.4426950408889634f : 1.0f;
            const int dst = ((which * H_ + head) * S_) * DH;
#pragma unroll
            for (int i = 0; i < 4; ++i) {
                const f32x4 u = acc[i][2 * b] * sc, v = acc[i][2 * b + 1] * sc;
                unsigned x0 = cvtpk(u[0], u[1]), x1 = cvtpk(u[2], u[3]), y0 = cvtpk(v[0], v[1]), y1 = cvtpk(v[2], v[3]);
                auto r0 = __builtin_amdgcn_permlane16_swap(x0, y0, false, false); auto r1 = __builtin_amdgcn_permlane16_swap(x1, y1, false, false);
                const int tok = mrow + 16 * i; int ch = 2 * (g & 1) + (g >> 1); if (which == 1) ch ^= (tok >> 2) & 3;
                st_wt((u32x4){r0[0], r1[0], r0[1], r1[1]}, rsq, (unsigned)(dst + tok * DH + ch * 8) * 2u);
            }
        }
        WAIT_BAR(0);
    } else {
        const rsrc_t rsa = mkrsrc(ao, S_ * LDAO * 2);
        u32x4 sv[14];
        auto pk = [&](const f32x4& a, const f32x4& b) { const f32x4 z = {0.f, 0.f, 0.f, 0.f}; const f32x4 u = __builtin_elementwise_max(a, z), v = __builtin_elementwise_max(b, z);
            unsigned x0 = cvtpk(u[0], u[1]), x1 = cvtpk(u[2], u[3]), y0 = cvtpk(v[0], v[1]), y1 = cvtpk(v[2], v[3]);
            auto r0 = __builtin_amdgcn_permlane16_swap(x0, y0, false, false); auto r1 = __builtin_amdgcn_permlane16_swap(x1, y1, false, false); return (u32x4){r0[0], r1[0], r0[1], r1[1]}; };
        auto off = [&](int s, int half) -> unsigned {
            const int c0 = n0 + 112 * half;
            if (s < 12) return (unsigned)((mrow + 16 * (s & 3)) * LDAO + c0 + 16 * half + 32 * (s >> 2) + 16 * (g & 1) + 8 * (g >> 1)) * 2u;
            return (unsigned)((mrow + 16 * (2 * (s - 12) + (g & 1))) * LDAO + c0 + (half ? 0 : 96) + 8 * (g >> 1)) * 2u; };
        WAIT_BAR(0);
        if (wn == 0) {
#pragma unroll
            for (int b = 0; b < 3; ++b)
#pragma unroll
                for (int i = 0; i < 4; ++i) sv[4 * b + i] = pk(acc[i][2 * b], acc[i][2 * b + 1]);
#pragma unroll
            for (int i = 0; i < 2; ++i) sv[12 + i] = pk(acc[2 * i][6], acc[2 * i + 1][6]);
#pragma unroll
            for (int s = 0; s < 14; ++s) *(LAS u32x4*)(lds + HSTG + (wm * 14 + s) * 1024 + lane * 16) = sv[s];
            LGKM_BAR();
        } else {
#pragma unroll
            for (int b = 0; b < 3; ++b)
#pragma unroll
                for (int i = 0; i < 4; ++i) sv[4 * b + i] = pk(acc[i][2 * b + 1], acc[i][2 * b + 2]);
#pragma unroll
            for (int i = 0; i < 2; ++i) sv[12 + i] = pk(acc[2 * i][0], acc[2 * i + 1][0]);
#pragma unroll
            for (int s = 0; s < 14; ++s) if (!EXP_NOHST) st_wt(sv[s], rsa, off(s, 1));
            LGKM_BAR();
#pragma unroll
            for (int s = 0; s < 14; ++s) sv[s] = *(const LAS u32x4*)(lds + HSTG + (wm * 14 + s) * 1024 + lane * 16);
#pragma unroll
            for (int s = 0; s < 14; ++s) if (!EXP_NOHST || sv[s][0] == 0x12345678) st_wt(sv[s], rsa, off(s, 0));
        }
    }
    STAMP(1, 3);
}

struct G2 { rsrc_t rA, rW2, rWo; unsigned aoff, boff[2]; int kh; };
__device__ __forceinline__ G2 g2_setup(const bf16_t* ao, const bf16_t* w2, const bf16_t* wo, int rb, int cb, int kh) {
    const int lane = threadIdx.x & 63, wid = __builtin_amdgcn_readfirstlane(threadIdx.x >> 6);
    G2 s; s.kh = kh;
    s.rA = mkrsrc(ao + (size_t)rb * 128 * LDAO + kh * 2304, (unsigned)(128 * LDAO - kh * 2304) * 2u);
    s.rW2 = mkrsrc(w2 + cb * 128, (unsigned)(DFF * D_ - cb * 128) * 2u); s.rWo = mkrsrc(wo + cb * 128, (unsigned)(D_ * D_ - cb * 128) * 2u);
    s.aoff = (unsigned)((16 * wid + (lane >> 3)) * LDAO + (((lane & 7) ^ ((lane >> 3) & 7)) * 8)) * 2u;
#pragma unroll
    for (int i = 0; i < 2; ++i) { const int k = 8 * wid + 4 * i + (lane >> 4), cp = lane & 15, swz = 2 * ((k & 3) | (((k >> 3) & 1) << 2)); s.boff[i] = (unsigned)(k * D_ + (cp ^ swz) * 8) * 2u; }
    return s;
}
__device__ __forceinline__ int g2_slot(int t) { return (int)((0x10002030u >> (8 * (t & 3))) & 0xffu) * 2048; }
__device__ __forceinline__ void g2_stage(lds_ptr lds, const G2& s, int t, int sl) {
    const int wid = __builtin_amdgcn_readfirstlane(threadIdx.x >> 6), gt = s.kh * 36 + t; const lds_ptr d = lds + g2_slot(sl);
#pragma unroll
    for (int i = 0; i < 2; ++i) bl_lds_dev(s.rA, d + (16 * wid + 8 * i) * 128, s.aoff, t * 128 + i * (8 * LDAO * 2));
    if (gt < 56) {
#pragma unroll
        for (int i = 0; i < 2; ++i) bl_lds(s.rW2, d + 16384 + (8 * wid + 4 * i) * 256, s.boff[i], gt * (64 * D_ * 2));
    } else {
#pragma unroll
        for (int i = 0; i < 2; ++i) bl_lds(s.rWo, d + 16384 + (8 * wid + 4 * i) * 256, s.boff[i], (gt - 56) * (64 * D_ * 2));
    }
}

constexpr int AT_KT = 128, AT_STAGE = 4 * AT_KT * 64, AT_NST = 1024 / AT_KT, AT_Q = 122880, AT_FL = 139264;
__device__ __forceinline__ void attn_body(lds_ptr lds, const bf16_t* qkv, bf16_t* ao, unsigned* flags, const int head, const int qb, const int hpub, const int hwait, const G2& g2) {
    const int tid = threadIdx.x, lane = tid & 63, wid = __builtin_amdgcn_readfirstlane(tid >> 6), wq = wid & 3, kh = wid >> 2;
    const int r32 = lane & 31, hi = lane >> 5;
    const rsrc_t rs = mkrsrc(qkv, 3 * S_ * D_ * 2);
    const unsigned ssrc = (unsigned)((((1 + (wid & 1)) * H_ + head) * S_ + (wid >> 1) * 1024) * 64);
    auto stage = [&](int st, int slot) {
#pragma unroll
        for (int i = 0; i < 8; ++i) bl_lds_dev(rs, lds + slot * AT_STAGE + (wid & 3) * 8192 + i * 1024, lane * 16, ssrc + st * (AT_KT * 64) + i * 1024); };
    STAMP(2, 0);
    if (wid < 4) {
        const int l = lane < 3 ? lane : 0;
        poll_acquire(flags, F_Q + (l * 1024 + head * 32) / 192, 8, 3);
        STAMP(2, 1);
#pragma unroll
        for (int i = 0; i < 4; ++i) bl_lds_dev(rs, lds + AT_Q + wid * 4096 + i * 1024, lane * 16, (unsigned)((head * S_ + qb * 256) * 64 + wid * 4096 + i * 1024));
    }
    const int kr[2] = {r32 * 64 + ((hi ^ ((r32 >> 2) & 3)) * 16), r32 * 64 + (((2 + hi) ^ ((r32 >> 2) & 3)) * 16)};
    const int vr = AT_KT * 64 + (4 * hi + ((lane & 15) >> 2)) * 64 + ((lane >> 4) & 1) * 32 + (lane & 3) * 8;
    bf16x8 sel;
    { const short one = ((lane & 15) == ((lane >> 4) & 1)) ? (short)0x3f80 : (short)0;
#pragma unroll
      for (int j = 0; j < 8; ++j) sel[j] = one; }
    bf16x8 qf[2][2];
    float mref[2];
    f32x16 o[2]; f32x4 ls[2];
    for (int attempt = 0; attempt < 2; ++attempt) {
        if (wid < 4) { stage(0, 0); stage(1, 1); asm volatile("s_waitcnt vmcnt(8)" ::: "memory"); STAMP(2, 2); WAIT_BAR(8); } else LGKM_BAR();
        if (attempt == 0) {
#pragma unroll
            for (int a = 0; a < 2; ++a)
#pragma unroll
                for (int s = 0; s < 2; ++s) qf[a][s] = lds_b128(lds + AT_Q + (wq * 64 + a * 32 + r32) * 64 + (2 * s + hi) * 16);
            const bf16x8 k0 = lds_b128(lds + kr[0]), k1 = lds_b128(lds + kr[1]);
#pragma unroll
            for (int a = 0; a < 2; ++a) { f32x16 s = {}; s = MFMA32(k0, qf[a][0], s); s = MFMA32(k1, qf[a][1], s);
                float m = s[0];
#pragma unroll
                for (int r = 1; r < 16; ++r) m = fmaxf(m, s[r]);
                auto sw = __builtin_amdgcn_permlane32_swap(__float_as_uint(m), __float_as_uint(m), false, false);
                mref[a] = fmaxf(__uint_as_float(sw[0]), __uint_as_float(sw[1])); }
        }
        f32x16 nm[2];
#pragma unroll
        for (int a = 0; a < 2; ++a) {
#pragma unroll
            for (int r = 0; r < 16; ++r) nm[a][r] = -mref[a];
            o[a] = (f32x16){}; ls[a] = (f32x4){}; }
        int slot = 0;
        STAMP(2, 3);
        for (int st = 0; st < AT_NST; ++st) {
            if (wid < 4 && st + 2 < AT_NST) stage(st + 2, (slot == 0) ? 2 : slot - 1);
            if (attempt == 0 && st == AT_NST - 1) g2_stage(lds, g2, 0, 0);
            const lds_ptr base = lds + slot * AT_STAGE + kh * (2 * AT_KT * 64);
#pragma unroll
            for (int kt = 0; kt < AT_KT / 32; ++kt) {
                const bf16x8 k0 = lds_b128(base + kt * 2048 + kr[0]), k1 = lds_b128(base + kt * 2048 + kr[1]);
                const bf16x8 v0 = cat4(lds_tr(base + kt * 2048 + vr), lds_tr(base + kt * 2048 + vr + 512)), v1 = cat4(lds_tr(base + kt * 2048 + vr + 1024), lds_tr(base + kt * 2048 + vr + 1536));
#pragma unroll
                for (int a = 0; a < 2; ++a) {
                    f32x16 s = MFMA32(k0, qf[a][0], nm[a]); s = MFMA32(k1, qf[a][1], s);
                    u32x4 p0, p1;
#pragma unroll
                    for (int r = 0; r < 4; ++r) { p0[r] = cvtpk(__builtin_amdgcn_exp2f(s[2 * r]), __builtin_amdgcn_exp2f(s[2 * r + 1])); p1[r] = cvtpk(__builtin_amdgcn_exp2f(s[8 + 2 * r]), __builtin_amdgcn_exp2f(s[8 + 2 * r + 1])); }
                    const bf16x8 pf0 = __builtin_bit_cast(bf16x8, p0), pf1 = __builtin_bit_cast(bf16x8, p1);
                    o[a] = MFMA32(v0, pf0, o[a]); o[a] = MFMA32(v1, pf1, o[a]);
                    ls[a] = MFMA16(sel, pf0, ls[a]); ls[a] = MFMA16(sel, pf1, ls[a]);
                }
            }
            if (wid < 4) {
                if (attempt == 0 && st == 5 && wid == 0) poll_acquire(flags, hwait, 16, 1);
                if (st + 2 < AT_NST) WAIT_BAR(8); else if (st + 1 < AT_NST) WAIT_BAR(0); else LGKM_BAR();
            } else { if (st == 2) WAIT_BAR(0); else LGKM_BAR(); }
            if (attempt == 0 && st == 2 && hpub >= 0) publish(flags, hpub);
            slot = (slot == 2) ? 0 : slot + 1;
        }
        STAMP(2, 4);
        if (attempt == 1) break;
        bool bad = false;
#pragma unroll
        for (int a = 0; a < 2; ++a) { bad |= !(ls[a][0] < 1.0e18f) || !(ls[a][1] < 1.0e18f);
#pragma unroll
            for (int r = 0; r < 16; ++r) bad |= !(fabsf(o[a][r]) < 1.0e30f); }
        LAS unsigned* fl = (LAS unsigned*)(lds + AT_FL);
        if (lane == 0) fl[wid] = __any(bad) ? 1u : 0u;
        LGKM_BAR();
        const u32x4 f0 = *(const LAS u32x4*)fl, f1 = *(const LAS u32x4*)(fl + 4);
        if ((f0[0] | f0[1] | f0[2] | f0[3] | f1[0] | f1[1] | f1[2] | f1[3]) == 0u) break;
        float mx[2] = {-INFINITY, -INFINITY};
        for (int st = 0; st < AT_NST; ++st) {
            if (wid < 4) stage(st, 0);
            WAIT_BAR(0);
#pragma unroll
            for (int h2 = 0; h2 < 2; ++h2)
                for (int kt = 0; kt < AT_KT / 32; ++kt) {
                    const lds_ptr base = lds + h2 * (2 * AT_KT * 64) + kt * 2048;
                    const bf16x8 k0 = lds_b128(base + kr[0]), k1 = lds_b128(base + kr[1]);
#pragma unroll
                    for (int a = 0; a < 2; ++a) { f32x16 s = {}; s = MFMA32(k0, qf[a][0], s); s = MFMA32(k1, qf[a][1], s);
#pragma unroll
                        for (int r = 0; r < 16; ++r) mx[a] = fmaxf(mx[a], s[r]); }
                }
            LGKM_BAR();
        }
#pragma unroll
        for (int a = 0; a < 2; ++a) { auto sw = __builtin_amdgcn_permlane32_swap(__float_as_uint(mx[a]), __float_as_uint(mx[a]), false, false);
            mref[a] = fmaxf(__uint_as_float(sw[0]), __uint_as_float(sw[1])); }
    }
    g2_stage(lds, g2, 1, 1);
    LAS float* xo = (LAS float*)lds + wq * (2 * 17 * 64);
    if (kh == 1) {
#pragma unroll
        for (int a = 0; a < 2; ++a) {
#pragma unroll
            for (int r = 0; r < 16; ++r) xo[(a * 17 + r) * 64 + lane] = o[a][r];
            if (lane < 16) { xo[(a * 17 + 16) * 64 + lane] = ls[a][0]; xo[(a * 17 + 16) * 64 + 16 + lane] = ls[a][1]; } }
    } else {
#pragma unroll
        for (int a = 0; a < 2; ++a) if (lane < 16) { xo[(a * 17 + 16) * 64 + 32 + lane] = ls[a][0]; xo[(a * 17 + 16) * 64 + 48 + lane] = ls[a][1]; }
    }
    LGKM_BAR();
    if (kh == 0) {
        const rsrc_t rsa = mkrsrc(ao, S_ * LDAO * 2);
#pragma unroll
        for (int a = 0; a < 2; ++a) {
            const float l = xo[(a * 17 + 16) * 64 + r32] + xo[(a * 17 + 16) * 64 + 32 + r32];
            const float rl = 1.0f / l;
            unsigned pk[8];
#pragma unroll
            for (int r = 0; r < 8; ++r) pk[r] = cvtpk((o[a][2 * r] + xo[(a * 17 + 2 * r) * 64 + lane]) * rl, (o[a][2 * r + 1] + xo[(a * 17 + 2 * r + 1) * 64 + lane]) * rl);
            const int dst = (qb * 256 + wq * 64 + a * 32 + r32) * LDAO + DFF + head * DH;
#pragma unroll
            for (int k = 0; k < 4; k += 2) {
                auto r0 = __builtin_amdgcn_permlane32_swap(pk[2 * k], pk[2 * k + 2], false, false); auto r1 = __builtin_amdgcn_permlane32_swap(pk[2 * k + 1], pk[2 * k + 3], false, false);
                st_wt((u32x4){r0[0], r1[0], r0[1], r1[1]}, rsa, (unsigned)(dst + 8 * k + 8 * hi) * 2u);
            }
        }
    }
    LGKM_BAR();
    STAMP(2, 5);
}

__device__ __forceinline__ void gemm2_body(lds_ptr lds, const G2& g2, const bf16_t* __restrict__ x, bf16_t* __restrict__ out, float* part, unsigned* flags, const int rb, const int cb, const int kh, const int opub) {
    constexpr int NKT = 36;
    const int tid = threadIdx.x, lane = tid & 63, wid = __builtin_amdgcn_readfirstlane(tid >> 6), kg = wid >> 2, wm = (wid >> 1) & 1, wn = wid & 1;
    const int fr = lane & 15, g = lane >> 4, q = fr >> 2, p = lane & 3;
    const int m0 = rb * 128, n0 = cb * 128;
    const int ar = (64 * wm + fr) * 128 + (((4 * kg + g) ^ (fr & 7)) * 16);
    int br[4];
    { const int swz = 2 * (q | ((g & 1) << 2));
#pragma unroll
      for (int j = 0; j < 4; ++j) br[j] = 16384 + (32 * kg + 8 * g + q) * 256 + (((2 * (4 * wn + j) + (p >> 1)) ^ swz) * 16) + (p & 1) * 8; }
    f32x4 acc[4][4];
#pragma unroll
    for (int i = 0; i < 4; ++i)
#pragma unroll
        for (int j = 0; j < 4; ++j) acc[i][j] = (f32x4){0.f, 0.f, 0.f, 0.f};
    bf16x8 A0[4], A1[4]; s16x4 B0[4][2], B1[4][2];
    const int wid4 = wid;
    auto st1 = [&](int t, int sl, int i) {
        const int gt = kh * 36 + t; const lds_ptr d = lds + g2_slot(sl);
        if (i < 2) bl_lds_dev(g2.rA, d + (16 * wid4 + 8 * i) * 128, g2.aoff, t * 128 + i * (8 * LDAO * 2));
        else { const bool w = gt < 56; bl_lds(w ? g2.rW2 : g2.rWo, d + 16384 + (8 * wid4 + 4 * (i - 2)) * 256, g2.boff[i - 2], (w ? gt : gt - 56) * (64 * D_ * 2)); } };
#define RDONE(AN, BN_, so, r) do { if ((r) < 4) AN[(r) & 3] = lds_b128(lds + (so) + ar + ((r) & 3) * 2048); \
        else if ((r) < 12) BN_[(((r) - 4) >> 1) & 3][(r) & 1] = lds_tr(lds + (so) + br[(((r) - 4) >> 1) & 3] + ((r) & 1) * 1024); } while (0)
#define STEP(AC, BC, AN, BN_, so, tl, sl) do { \
        unroll<0, 16>([&](auto ic) { constexpr int idx = decltype(ic)::value; constexpr int j = idx >> 2, i = idx & 3; \
            acc[i][j] = MFMA16(cat4(BC[j][0], BC[j][1]), AC[i], acc[i][j]); SB0(); \
            RDONE(AN, BN_, so, idx); \
            if ((idx & 3) == 2) st1(tl, sl, idx >> 2); \
            SB0(); }); } while (0)
    STAMP(3, 0);
    g2_stage(lds, g2, 2, 2); g2_stage(lds, g2, 3, 3);
    WAIT_BAR(8);
    publish(flags, opub);
    STAMP(3, 1);
    unroll<0, 12>([&](auto ic) { constexpr int idx = decltype(ic)::value; RDONE(A0, B0, g2_slot(0), idx); });
    WAIT_BAR(8);
    for (int t = 0; t < NKT; t += 2) {
        if (kh == 1 && t == 12 && wid == 0) poll_acquire(flags, F_O + (rb >> 1), 32, 1);
        SB0();
        STEP(A0, B0, A1, B1, g2_slot(t + 1), min(t + 4, NKT - 1), t);
        WAIT_BAR(8); SB0();
        STEP(A1, B1, A0, B0, g2_slot(t + 2), min(t + 5, NKT - 1), t + 1);
        WAIT_BAR(8);
    }
#undef RDONE
#undef STEP
    WAIT_BAR(0);
    STAMP(3, 2);
    LAS f32x4* xr = (LAS f32x4*)lds + (wid & 3) * 1024;
#pragma unroll
    for (int i = 0; i < 2; ++i)
#pragma unroll
        for (int j = 0; j < 4; ++j) xr[(kg * 8 + i * 4 + j) * 64 + lane] = kg ? acc[i][j] : acc[2 + i][j];
    LGKM_BAR();
    f32x4 r[2][4];
#pragma unroll
    for (int i = 0; i < 2; ++i)
#pragma unroll
        for (int j = 0; j < 4; ++j) r[i][j] = (kg ? acc[2 + i][j] : acc[i][j]) + xr[((kg ^ 1) * 8 + i * 4 + j) * 64 + lane];
    const int tile = rb * 8 + cb;
    { const rsrc_t rs = mkrsrc(part + (size_t)(tile * 2 + kh) * 8192, 32768);
#pragma unroll
      for (int j = 0; j < 4; ++j) st_wt(__builtin_bit_cast(u32x4, kh ? r[0][j] : r[1][j]), rs, (wid * 4 + j) * 1024 + lane * 16); }
    WAIT_BAR(0);
    if (tid == 0) __hip_atomic_store(flags + (F_P + tile * 2 + kh) * 32, 1u, RLX_AGENT);
    const int mrow = m0 + 64 * wm + 32 * kg + 16 * kh + fr;
    u32x2 xv[4];
#pragma unroll
    for (int j = 0; j < 4; ++j) xv[j] = *(const u32x2*)(x + (size_t)mrow * D_ + n0 + 64 * wn + 16 * j + 4 * g);
    if (wid == 0) { unsigned spins = 0;
        while (__builtin_amdgcn_readfirstlane(__hip_atomic_load(flags + (F_P + tile * 2 + (kh ^ 1)) * 32, RLX_AGENT)) == 0u) { __builtin_amdgcn_s_sleep(1); if (++spins > (1u << 22)) break; } }
    __syncthreads();
    STAMP(3, 3);
    { const rsrc_t rs = mkrsrc(part + (size_t)(tile * 2 + (kh ^ 1)) * 8192, 32768);
      f32x4 o[4];
#pragma unroll
      for (int j = 0; j < 4; ++j) o[j] = __builtin_bit_cast(f32x4, __builtin_amdgcn_raw_buffer_load_b128(rs, (wid * 4 + j) * 1024 + lane * 16, 0, 16));
      unsigned pk[4][2];
#pragma unroll
      for (int j = 0; j < 4; ++j) { f32x4 v = (kh ? r[1][j] : r[0][j]) + o[j];
          v[0] += __uint_as_float(xv[j][0] << 16); v[1] += __uint_as_float(xv[j][0] & 0xffff0000u); v[2] += __uint_as_float(xv[j][1] << 16); v[3] += __uint_as_float(xv[j][1] & 0xffff0000u);
          pk[j][0] = cvtpk(v[0], v[1]); pk[j][1] = cvtpk(v[2], v[3]); }
#pragma unroll
      for (int j = 0; j < 4; j += 2) {
          auto r0 = __builtin_amdgcn_permlane16_swap(pk[j][0], pk[j + 1][0], false, false); auto r1 = __builtin_amdgcn_permlane16_swap(pk[j][1], pk[j + 1][1], false, false);
          *(u32x4*)(out + (size_t)mrow * D_ + n0 + 64 * wn + 16 * (j + (g & 1)) + 8 * (g >> 1)) = (u32x4){r0[0], r1[0], r0[1], r1[1]}; } }
    STAMP(3, 4);
}

__global__ __launch_bounds__(512, 2) void fused_kernel(const bf16_t* __restrict__ xn, const bf16_t* __restrict__ wqkv, const bf16_t* __restrict__ w1, const bf16_t* __restrict__ w2, const bf16_t* __restrict__ wo,
                                                       const bf16_t* __restrict__ x, bf16_t* qkv, bf16_t* ao, bf16_t* __restrict__ out, float* part, unsigned* flags) {
    __shared__ __attribute__((aligned(16))) char smem[LDS_BYTES];
    const lds_ptr lds = (lds_ptr)smem;
    const int xg = blockIdx.x & 7, r = blockIdx.x >> 3;
    const int rb1 = (xg & 1) * 4 + (r & 3), c1 = (xg >> 1) * 4 + ((r >> 2) & 3), ffn = r >> 4;
    if (ffn) gemm1_body<7>(lds, xn, w1, DFF, rb1 * 256, c1 * 224, qkv, ao);
    else { gemm1_body<6>(lds, xn, wqkv, 3 * D_, rb1 * 256, c1 * 192, qkv, ao); publish(flags, F_Q + c1); }
    const int kh = xg & 1, rb3 = ((xg >> 1) & 1) * 8 + (r & 7), cb3 = (xg >> 2) * 4 + (r >> 3);
    const G2 g2 = g2_setup(ao, w2, wo, rb3, cb3, kh);
    const int head = xg * 4 + (r >> 3), qb = r & 7;
    attn_body(lds, qkv, ao, flags, head, qb, ffn ? F_H + rb1 : -1, F_H + (rb3 >> 1), g2);
    gemm2_body(lds, g2, x, out, part, flags, rb3, cb3, kh, F_O + qb);
}

extern "C" void kernel_launch(void* const* d_in, const int* in_sizes, int n_in, void* d_out, int out_size, void* d_ws, size_t ws_size, hipStream_t stream) {
    const bf16_t* x = (const bf16_t*)d_in[0]; const bf16_t* wqkv = (const bf16_t*)d_in[1]; const bf16_t* wo = (const bf16_t*)d_in[2];
    const bf16_t* w1 = (const bf16_t*)d_in[4]; const bf16_t* w2 = (const bf16_t*)d_in[5];
    char* ws = (char*)d_ws;
    unsigned* flags = (unsigned*)(ws + WS_FLAGS); bf16_t* xn = (bf16_t*)(ws + WS_XN); bf16_t* qkv = (bf16_t*)(ws + WS_QKV); bf16_t* ao = (bf16_t*)(ws + WS_AO); float* part = (float*)(ws + WS_PART);
    ln_kernel<<<256, 512, 0, stream>>>(x, xn, flags);
    fused_kernel<<<256, 512, 0, stream>>>(xn, wqkv, w1, w2, wo, x, qkv, ao, (bf16_t*)d_out, part, flags);
}
```

```cpp
#include <hip/hip_runtime.h>
#include <hip/hip_bf16.h>
#include <stdint.h>

typedef unsigned short bf16_t;
typedef short bf16x8 __attribute__((ext_vector_type(8)));
typedef short s16x4 __attribute__((ext_vector_type(4)));
typedef float f32x2 __attribute__((ext_vector_type(2)));
typedef float f32x4 __attribute__((ext_vector_type(4)));
typedef float f32x16 __attribute__((ext_vector_type(16)));
typedef unsigned u32x4 __attribute__((ext_vector_type(4)));
typedef unsigned u32x2 __attribute__((ext_vector_type(2)));
typedef __bf16 bf16x2n __attribute__((ext_vector_type(2)));
typedef __bf16 bf16x4n __attribute__((ext_vector_type(4)));
#define LAS __attribute__((address_space(3)))
typedef LAS char* lds_ptr;

constexpr int S_ = 2048, D_ = 1024, H_ = 32, DH = 32, DFF = 3584;
constexpr int LDAO = DFF + D_;
constexpr size_t WS_FLAGS = 0;
constexpr size_t WS_QKV = 1u << 20;
constexpr size_t WS_AO = WS_QKV + (size_t)3 * S_ * D_ * 2;
constexpr size_t WS_PART = WS_AO + (size_t)S_ * LDAO * 2;
constexpr int LDS_BYTES = 163840;
constexpr int F_E = 0, F_ST = 256, F_Q = 1024, F_H = 2048, F_O = 3072, F_P = 4096;
constexpr int F_LT = 512, F_CT = 768;
constexpr int F_LN = 16384, F_CS = 32768;
constexpr int NCOL = 3 * D_ + DFF;
constexpr int TBL = 151552;

#define EXP_NOADD 0
#define EXP_NOROT 0
#define EXP_NOHST 0
#define STAMP(k, i) do {} while (0)

__device__ __forceinline__ unsigned cvtpk(float lo, float hi) { return __builtin_bit_cast(unsigned, __builtin_convertvector((f32x2){lo, hi}, bf16x2n)); }
__device__ __forceinline__ float rbf(float x) { return __uint_as_float(cvtpk(x, x) << 16); }
typedef __amdgpu_buffer_rsrc_t rsrc_t;
__device__ __forceinline__ rsrc_t mkrsrc(const void* p, unsigned bytes) { return __builtin_amdgcn_make_buffer_rsrc((void*)p, 0, bytes, 0x00020000); }
__device__ __forceinline__ void bl_lds(rsrc_t r, lds_ptr l, unsigned voff, unsigned soff) {
    asm volatile("s_mov_b32 m0, %0\n\ts_nop 0\n\tbuffer_load_dwordx4 %1, %2, %3 offen lds" :: "s"(__builtin_amdgcn_readfirstlane((unsigned)(uintptr_t)l)), "v"(voff), "s"(r), "s"(__builtin_amdgcn_readfirstlane(soff)) : "memory"); }
__device__ __forceinline__ void bl_lds_dev(rsrc_t r, lds_ptr l, unsigned voff, unsigned soff) {
    asm volatile("s_mov_b32 m0, %0\n\ts_nop 0\n\tbuffer_load_dwordx4 %1, %2, %3 offen sc1 lds" :: "s"(__builtin_amdgcn_readfirstlane((unsigned)(uintptr_t)l)), "v"(voff), "s"(r), "s"(__builtin_amdgcn_readfirstlane(soff)) : "memory"); }
__device__ __forceinline__ void st_wt(u32x4 v, rsrc_t r, unsigned off) { __builtin_amdgcn_raw_buffer_store_b128(v, r, off, 0, 16); }
__device__ __forceinline__ bf16x8 lds_b128(lds_ptr p) { return *(const LAS bf16x8*)p; }
__device__ __forceinline__ s16x4 lds_tr(lds_ptr p) { return __builtin_amdgcn_ds_read_tr16_b64_v4i16((LAS s16x4*)p); }
__device__ __forceinline__ bf16x8 cat4(s16x4 a, s16x4 b) { return (bf16x8){a[0], a[1], a[2], a[3], b[0], b[1], b[2], b[3]}; }
#define MFMA16(a, b, c) __builtin_amdgcn_mfma_f32_16x16x32_bf16(a, b, c, 0, 0, 0)
#define MFMA32(a, b, c) __builtin_amdgcn_mfma_f32_32x32x16_bf16(a, b, c, 0, 0, 0)
#define WAIT_BAR(N) asm volatile("s_waitcnt vmcnt(%0) lgkmcnt(0)\n\ts_barrier" :: "n"(N) : "memory")
#define LGKM_BAR() asm volatile("s_waitcnt lgkmcnt(0)\n\ts_barrier" ::: "memory")
#define SB0() __builtin_amdgcn_sched_barrier(0)
#define RLX_AGENT __ATOMIC_RELAXED, __HIP_MEMORY_SCOPE_AGENT
template <int I> struct IC { static constexpr int value = I; };
template <int B, int E, class F> __device__ __forceinline__ void unroll(F&& f) { if constexpr (B < E) { f(IC<B>{}); unroll<B + 1, E>(f); } }

__device__ __forceinline__ float wave_sum(float v) {
    v += __builtin_bit_cast(float, __builtin_amdgcn_update_dpp(0, __builtin_bit_cast(int, v), 0xB1, 0xf, 0xf, true));
    v += __builtin_bit_cast(float, __builtin_amdgcn_update_dpp(0, __builtin_bit_cast(int, v), 0x4E, 0xf, 0xf, true));
    v += __builtin_bit_cast(float, __builtin_amdgcn_update_dpp(0, __builtin_bit_cast(int, v), 0x141, 0xf, 0xf, true));
    v += __builtin_bit_cast(float, __builtin_amdgcn_update_dpp(0, __builtin_bit_cast(int, v), 0x140, 0xf, 0xf, true));
    { auto r = __builtin_amdgcn_permlane16_swap(__float_as_uint(v), __float_as_uint(v), false, false); v = __uint_as_float(r[0]) + __uint_as_float(r[1]); }
    { auto r = __builtin_amdgcn_permlane32_swap(__float_as_uint(v), __float_as_uint(v), false, false); v = __uint_as_float(r[0]) + __uint_as_float(r[1]); }
    return v;
}
__device__ __forceinline__ void poll_tags(unsigned* flags, int word, unsigned T, int n) {
    const bool act = (int)(threadIdx.x & 63) < n; unsigned spins = 0;
    for (;;) { unsigned v = T; if (act) v = __hip_atomic_load(flags + word, RLX_AGENT);
        if (__all(v == T)) break; __builtin_amdgcn_s_sleep(2); if (++spins > (1u << 22)) break; }
    asm volatile("" ::: "memory");
}
__device__ __forceinline__ void publish(unsigned* flags, int word, unsigned T) {
    if (threadIdx.x == 0) __hip_atomic_store(flags + word, T, RLX_AGENT);
}
typedef unsigned long long u64;
__device__ __forceinline__ void put_granule(unsigned* flags, int word, float v, unsigned T) { __hip_atomic_store((u64*)(flags + word), ((u64)T << 32) | __float_as_uint(v), RLX_AGENT); }
__device__ __forceinline__ u64 get_granule(unsigned* flags, int word) { return __hip_atomic_load((u64*)(flags + word), RLX_AGENT); }

constexpr int HSTG = 65536;
template <int NT>
__device__ __forceinline__ unsigned gemm1_body(lds_ptr lds, const bf16_t* xn, const bf16_t* W, const int ldw, const int m0, const int n0, bf16_t* qkv, bf16_t* ao, unsigned* flags, const int j32) {
    constexpr int BN = 32 * NT, RB = BN * 2, CPR = BN / 8, BSLOT = 64 * RB, NKT = D_ / 64, GB = (NT == 7) ? 4 : 3;
    constexpr int LB = 65536, NMF = 4 * NT, NRD = 4 + 2 * NT, NBB = (NT == 7) ? 2 : 4;
    const int tid = threadIdx.x, lane = tid & 63, wid = __builtin_amdgcn_readfirstlane(tid >> 6), wm = wid & 3, wn = wid >> 2;
    const int fr = lane & 15, g = lane >> 4, q = fr >> 2, p = lane & 3;
    const rsrc_t rA = mkrsrc(xn + (size_t)m0 * D_, 256 * D_ * 2), rB = mkrsrc(W + n0, (unsigned)(D_ * ldw - n0) * 2u);
    const unsigned aoff = (unsigned)((32 * wid + (lane >> 3)) * D_ + (((lane & 7) ^ ((lane >> 3) & 7)) * 8)) * 2u;
    unsigned boff[GB];
#pragma unroll
    for (int i = 0; i < GB; ++i) { const int idx = 64 * i + lane, rl = idx / CPR, cp = idx % CPR, k = (8 * wid + rl) & 63;
        const int swz = (NT == 7) ? 2 * ((k >> 3) & 1) : 2 * (((k >> 1) & 1) | (((k >> 3) & 1) << 1));
        boff[i] = (unsigned)(k * ldw + (cp ^ swz) * 8) * 2u; }
    const int krot = (m0 >> 10) * 8, tcs = ((m0 >> 8) & 3) * 2;
    auto stageA1 = [&](int kt_, int slot, int i) { const int kt = (kt_ + krot) & (NKT - 1); bl_lds(rA, lds + slot * 32768 + (32 * wid + 8 * i) * 128, aoff, kt * 128 + i * (8 * D_ * 2)); };
    auto stageB1 = [&](int kt_, int slot, int i) { const int kt = (kt_ + krot) & (NKT - 1); const unsigned so = (unsigned)(kt * 64 * ldw) * 2u;
        if (NT == 7 && i == 3) { if (lane < 32) bl_lds(rB, lds + LB + slot * BSLOT + (8 * wid * CPR + 192) * 16, boff[GB - 1], so); }
        else bl_lds(rB, lds + LB + slot * BSLOT + (8 * wid * CPR + 64 * i) * 16, boff[i < GB ? i : 0], so); };
    int ar[2];
#pragma unroll
    for (int ks = 0; ks < 2; ++ks) ar[ks] = (64 * wm + fr) * 128 + (((4 * ks + g) ^ (fr & 7)) * 16);
    int br[NBB];
    { const int swz = (NT == 7) ? 2 * (g & 1) : 2 * ((q >> 1) | ((g & 1) << 1));
#pragma unroll
      for (int j = 0; j < NBB; ++j) br[j] = LB + (8 * g + q) * RB + (((2 * (NT * wn + j) + (p >> 1)) ^ swz) * 16) + (p & 1) * 8; }
    f32x4 acc[4][NT];
#pragma unroll
    for (int i = 0; i < 4; ++i)
#pragma unroll
        for (int j = 0; j < NT; ++j) acc[i][j] = (f32x4){0.f, 0.f, 0.f, 0.f};
    bf16x8 A0[4], A1[4]; s16x4 B0[NT][2], B1[NT][2];
#define RDONE(AN, BN_, sa, sb, ks, r) do { if ((r) < 4) AN[(r) < 4 ? (r) : 0] = lds_b128(lds + (sa) + ar[ks] + ((r) < 4 ? (r) : 0) * 2048); \
        else if ((r) < NRD) { constexpr int jj = (((r) - 4) >> 1) < NT && (r) >= 4 ? (((r) - 4) >> 1) : 0, tt = ((r) - 4) & 1; BN_[jj][tt] = lds_tr(lds + (sb) + br[jj % NBB] + 32 * (jj - jj % NBB) + (32 * (ks) + 4 * tt) * RB); } } while (0)
#define SUBSTEP(AC, BC, AN, BN_, sa, sb, ks, STG, go, tl, sl) do { \
        unroll<0, NMF>([&](auto ic) { constexpr int idx = decltype(ic)::value; constexpr int j = idx >> 2, i = idx & 3; \
            acc[i][j] = MFMA16(cat4(BC[j][0], BC[j][1]), AC[i], acc[i][j]); SB0(); \
            RDONE(AN, BN_, sa, sb, ks, idx); \
            if ((idx % NT) == 1 && (go)) { if (STG == 1) stageA1(tl, sl, idx / NT); if (STG == 2 && idx / NT < GB) stageB1(tl, sl, idx / NT); } \
            SB0(); }); } while (0)
    STAMP(1, 0);
#pragma unroll
    for (int t = 0; t < 2; ++t) {
#pragma unroll
        for (int i = 0; i < GB; ++i) stageB1(t, t, i);
#pragma unroll
        for (int i = 0; i < 4; ++i) stageA1(t, t, i); }
    unsigned T; float lnv;
    const int ncg = (NT == 7 ? 3 * D_ : 0) + n0;
    {   const int row = m0 + 8 * j32 + wid;
        const u32x4* src = (const u32x4*)(xn + (size_t)row * D_);
        const u32x4 v0 = src[lane], v1 = src[64 + lane];
        const unsigned ev = __builtin_amdgcn_raw_buffer_load_b32(mkrsrc(flags, 4096), 0, F_E * 4, 16);
        SB0();
        float f[16];
#pragma unroll
        for (int i = 0; i < 4; ++i) { f[2 * i] = __uint_as_float(v0[i] << 16); f[2 * i + 1] = __uint_as_float(v0[i] & 0xffff0000u); f[8 + 2 * i] = __uint_as_float(v1[i] << 16); f[8 + 2 * i + 1] = __uint_as_float(v1[i] & 0xffff0000u); }
        float sm = 0.f;
#pragma unroll
        for (int i = 0; i < 16; ++i) sm += f[i];
        const float mean = wave_sum(sm) * (1.0f / D_);
        float qq = 0.f;
#pragma unroll
        for (int i = 0; i < 16; ++i) { const float d = f[i] - mean; qq += d * d; }
        qq = wave_sum(qq);
        T = (unsigned)__builtin_amdgcn_readfirstlane(ev) + 1u;
        const float sd = rbf(__fsqrt_rn(rbf(rbf(qq * (1.0f / D_)) + 1e-5f)));
        lnv = (lane & 1) ? 1.0f / sd : rbf(mean);
    }
    WAIT_BAR(4 + GB);
    auto colsum = [&](s16x4 (&BC)[NT][2]) {
        const bf16x8 ones = {0x3f80, 0x3f80, 0x3f80, 0x3f80, 0x3f80, 0x3f80, 0x3f80, 0x3f80}; const f32x4 z = {0.f, 0.f, 0.f, 0.f};
        float c[NT];
#pragma unroll
        for (int j = 0; j < NT; ++j) c[j] = MFMA16(ones, cat4(BC[j][0], BC[j][1]), z)[0];
        if (lane < 16) {
#pragma unroll
            for (int j = 0; j < NT; ++j) ((LAS float*)(lds + TBL + 2048))[wm * 256 + 16 * (NT * wn + j) + lane] = c[j]; } };
    STAMP(1, 1);
    unroll<0, NRD>([&](auto ic) { constexpr int idx = decltype(ic)::value; RDONE(A0, B0, 0, 0, 0, idx); });
    int sa = 0, sb = 0, sb3 = 0;
    const rsrc_t rF = mkrsrc(flags, 1u << 20);
    unsigned long long itv = 0;
    const unsigned evmask = 1u | (1u << (tcs + (wm >> 1))) | (1u << (tcs + 3)) | (1u << (NKT - 2)) | (wid == 0 ? (4u | (1u << (tcs + 5)) | (1u << (NKT - 3))) : 0u);
    for (int t = 0; t < NKT; ++t) {
        const int tn = min(t + 2, NKT - 1); constexpr bool go = true;
        SB0();
        { const int slb = (sb3 == 0) ? 2 : sb3 - 1;
          SUBSTEP(A0, B0, A1, B1, sa, sb, 1, 2, go, tn, slb); }
        if (__builtin_expect((evmask >> t) & 1u, 0)) {
            if (t == 0) { if (lane < 2) __hip_atomic_store(flags + F_LN + (m0 + 8 * j32 + wid) * 2 + lane, __float_as_uint(lnv), RLX_AGENT);
                if (wid == 0) { publish(flags, F_ST + blockIdx.x, T); WAIT_BAR(GB + 2); } else WAIT_BAR(GB + 1); }
            else if (wid == 0 && (t == 2 || t == tcs + 5)) { publish(flags, (t == 2 ? F_LT : F_CT) + (m0 >> 8) * 32 + j32, T); WAIT_BAR(GB + 1); }
            else if (wid * 64 < BN && t == tcs + 3) { if (tid < BN) { const LAS float* pc = (const LAS float*)(lds + TBL + 2048) + tid;
                    __hip_atomic_store(flags + F_CS + (m0 >> 8) * NCOL + ncg + tid, __float_as_uint((pc[0] + pc[256]) + (pc[512] + pc[768])), RLX_AGENT); }
                WAIT_BAR(GB + 1); }
            else WAIT_BAR(GB);
            if (t == tcs + (wm >> 1)) { if (wm & 1) colsum(B1); else colsum(B0); }
            if (t == NKT - 3 && wid == 0) poll_tags(flags, lane < 32 ? F_LT + (m0 >> 8) * 32 + lane : F_CT + (lane & 7) * 32 + j32, T, 40);
            if (t == NKT - 2) {
                if (lane < BN / 4) bl_lds_dev(rF, lds + TBL + 2048 + wid * 1024, lane * 16, (F_CS + wid * NCOL + ncg) * 4);
                if (wid < 2) bl_lds_dev(rF, lds + TBL + wid * 1024, lane * 16, (F_LN + m0 * 2) * 4 + wid * 1024); }
        } else WAIT_BAR(GB);
        SB0();
        sa ^= 32768; sb3 = (sb3 == 2) ? 0 : sb3 + 1; sb = sb3 * BSLOT;
        SUBSTEP(A1, B1, A0, B0, sa, sb, 0, 1, go, tn, t & 1);
    }
#undef RDONE
#undef SUBSTEP
    STAMP(1, 2);
    if (tid < BN) { float cs = 0.f;
#pragma unroll
        for (int i = 0; i < 8; ++i) cs += ((const LAS float*)(lds + TBL + 2048))[i * 256 + tid];
        ((LAS float*)(lds + TBL + 10240))[tid] = cs; }
    WAIT_BAR(0);
#pragma unroll
    for (int i = 0; i < 4; ++i) { const f32x2 st = ((const LAS f32x2*)(lds + TBL))[64 * wm + 16 * i + fr];
#pragma unroll
        for (int j = 0; j < NT; ++j) { const f32x4 cs = *(const LAS f32x4*)(lds + TBL + 10240 + (16 * (NT * wn + j) + 4 * g) * 4); acc[i][j] = (acc[i][j] - cs * st[0]) * st[1]; } }
    const int mrow = m0 + 64 * wm + fr;
    if constexpr (NT == 6) {
        const rsrc_t rsq = mkrsrc(qkv, 3 * S_ * D_ * 2);
#pragma unroll
        for (int b = 0; b < 3; ++b) {
            const int col = n0 + 96 * wn + 32 * b, which = col >> 10, head = (col & 1023) >> 5;
            const float sc = (which == 0) ? 0.17677669529663687f * 1.4426950408889634f : 1.0f;
            const int dst = ((which * H_ + head) * S_) * DH;
#pragma unroll
            for (int i = 0; i < 4; ++i) {
                const f32x4 u = acc[i][2 * b] * sc, v = acc[i][2 * b + 1] * sc;
                unsigned x0 = cvtpk(u[0], u[1]), x1 = cvtpk(u[2], u[3]), y0 = cvtpk(v[0], v[1]), y1 = cvtpk(v[2], v[3]);
                auto r0 = __builtin_amdgcn_permlane16_swap(x0, y0, false, false); auto r1 = __builtin_amdgcn_permlane16_swap(x1, y1, false, false);
                const int tok = mrow + 16 * i; int ch = 2 * (g & 1) + (g >> 1); if (which == 1) ch ^= (tok >> 2) & 3;
                st_wt((u32x4){r0[0], r1[0], r0[1], r1[1]}, rsq, (unsigned)(dst + tok * DH + ch * 8) * 2u);
            }
        }
        WAIT_BAR(0);
    } else {
        const rsrc_t rsa = mkrsrc(ao, S_ * LDAO * 2);
        u32x4 sv[14];
        auto pk = [&](const f32x4& a, const f32x4& b) { const f32x4 z = {0.f, 0.f, 0.f, 0.f}; const f32x4 u = __builtin_elementwise_max(a, z), v = __builtin_elementwise_max(b, z);
            unsigned x0 = cvtpk(u[0], u[1]), x1 = cvtpk(u[2], u[3]), y0 = cvtpk(v[0], v[1]), y1 = cvtpk(v[2], v[3]);
            auto r0 = __builtin_amdgcn_permlane16_swap(x0, y0, false, false); auto r1 = __builtin_amdgcn_permlane16_swap(x1, y1, false, false); return (u32x4){r0[0], r1[0], r0[1], r1[1]}; };
        auto off = [&](int s, int half) -> unsigned {
            const int c0 = n0 + 112 * half;
            if (s < 12) return (unsigned)((mrow + 16 * (s & 3)) * LDAO + c0 + 16 * half + 32 * (s >> 2) + 16 * (g & 1) + 8 * (g >> 1)) * 2u;
            return (unsigned)((mrow + 16 * (2 * (s - 12) + (g & 1))) * LDAO + c0 + (half ? 0 : 96) + 8 * (g >> 1)) * 2u; };
        if (wn == 0) {
#pragma unroll
            for (int b = 0; b < 3; ++b)
#pragma unroll
                for (int i = 0; i < 4; ++i) sv[4 * b + i] = pk(acc[i][2 * b], acc[i][2 * b + 1]);
#pragma unroll
            for (int i = 0; i < 2; ++i) sv[12 + i] = pk(acc[2 * i][6], acc[2 * i + 1][6]);
#pragma unroll
            for (int s = 0; s < 14; ++s) *(LAS u32x4*)(lds + HSTG + (wm * 14 + s) * 1024 + lane * 16) = sv[s];
            LGKM_BAR();
        } else {
#pragma unroll
            for (int b = 0; b < 3; ++b)
#pragma unroll
                for (int i = 0; i < 4; ++i) sv[4 * b + i] = pk(acc[i][2 * b + 1], acc[i][2 * b + 2]);
#pragma unroll
            for (int i = 0; i < 2; ++i) sv[12 + i] = pk(acc[2 * i][0], acc[2 * i + 1][0]);
#pragma unroll
            for (int s = 0; s < 14; ++s) if (!EXP_NOHST) st_wt(sv[s], rsa, off(s, 1));
            LGKM_BAR();
#pragma unroll
            for (int s = 0; s < 14; ++s) sv[s] = *(const LAS u32x4*)(lds + HSTG + (wm * 14 + s) * 1024 + lane * 16);
#pragma unroll
            for (int s = 0; s < 14; ++s) if (!EXP_NOHST || sv[s][0] == 0x12345678) st_wt(sv[s], rsa, off(s, 0));
        }
    }
    STAMP(1, 3);
    return T;
}

struct G2 { rsrc_t rA, rW2, rWo; unsigned aoff, boff[2]; int kh; };
__device__ __forceinline__ G2 g2_setup(const bf16_t* ao, const bf16_t* w2, const bf16_t* wo, int rb, int cb, int kh) {
    const int lane = threadIdx.x & 63, wid = __builtin_amdgcn_readfirstlane(threadIdx.x >> 6);
    G2 s; s.kh = kh;
    s.rA = mkrsrc(ao + (size_t)rb * 128 * LDAO + kh * 2304, (unsigned)(128 * LDAO - kh * 2304) * 2u);
    s.rW2 = mkrsrc(w2 + cb * 128, (unsigned)(DFF * D_ - cb * 128) * 2u); s.rWo = mkrsrc(wo + cb * 128, (unsigned)(D_ * D_ - cb * 128) * 2u);
    s.aoff = (unsigned)((16 * wid + (lane >> 3)) * LDAO + (((lane & 7) ^ ((lane >> 3) & 7)) * 8)) * 2u;
#pragma unroll
    for (int i = 0; i < 2; ++i) { const int k = 8 * wid + 4 * i + (lane >> 4), cp = lane & 15, swz = 2 * ((k & 3) | (((k >> 3) & 1) << 2)); s.boff[i] = (unsigned)(k * D_ + (cp ^ swz) * 8) * 2u; }
    return s;
}
__device__ __forceinline__ int g2_slot(int t) { return (int)((0x10002030u >> (8 * (t & 3))) & 0xffu) * 2048; }
__device__ __forceinline__ void g2_stage(lds_ptr lds, const G2& s, int t, int sl) {
    const int wid = __builtin_amdgcn_readfirstlane(threadIdx.x >> 6), gt = s.kh * 36 + t; const lds_ptr d = lds + g2_slot(sl);
#pragma unroll
    for (int i = 0; i < 2; ++i) bl_lds_dev(s.rA, d + (16 * wid + 8 * i) * 128, s.aoff, t * 128 + i * (8 * LDAO * 2));
    if (gt < 56) {
#pragma unroll
        for (int i = 0; i < 2; ++i) bl_lds(s.rW2, d + 16384 + (8 * wid + 4 * i) * 256, s.boff[i], gt * (64 * D_ * 2));
    } else {
#pragma unroll
        for (int i = 0; i < 2; ++i) bl_lds(s.rWo, d + 16384 + (8 * wid + 4 * i) * 256, s.boff[i], (gt - 56) * (64 * D_ * 2));
    }
}

constexpr int AT_KT = 128, AT_STAGE = 4 * AT_KT * 64, AT_NST = 1024 / AT_KT, AT_Q = 122880, AT_FL = 139264;
__device__ __forceinline__ void attn_body(lds_ptr lds, const bf16_t* qkv, bf16_t* ao, unsigned* flags, const unsigned T, const int head, const int qb, const int hpub, const int hwait, const G2& g2) {
    const int tid = threadIdx.x, lane = tid & 63, wid = __builtin_amdgcn_readfirstlane(tid >> 6), wq = wid & 3, kh = wid >> 2;
    const int r32 = lane & 31, hi = lane >> 5;
    const rsrc_t rs = mkrsrc(qkv, 3 * S_ * D_ * 2);
    const unsigned ssrc = (unsigned)((((1 + (wid & 1)) * H_ + head) * S_ + (wid >> 1) * 1024) * 64);
    auto stage = [&](int st, int slot) {
#pragma unroll
        for (int i = 0; i < 8; ++i) bl_lds_dev(rs, lds + slot * AT_STAGE + (wid & 3) * 8192 + i * 1024, lane * 16, ssrc + st * (AT_KT * 64) + i * 1024); };
    STAMP(2, 0);
    if (wid < 4) {
        poll_tags(flags, F_Q + (((lane >> 3) & 3) * 1024 + head * 32) / 192 * 32 + (lane & 7), T, 24);
        STAMP(2, 1);
#pragma unroll
        for (int i = 0; i < 4; ++i) bl_lds_dev(rs, lds + AT_Q + wid * 4096 + i * 1024, lane * 16, (unsigned)((head * S_ + qb * 256) * 64 + wid * 4096 + i * 1024));
    }
    const int kr[2] = {r32 * 64 + ((hi ^ ((r32 >> 2) & 3)) * 16), r32 * 64 + (((2 + hi) ^ ((r32 >> 2) & 3)) * 16)};
    const int vr = AT_KT * 64 + (4 * hi + ((lane & 15) >> 2)) * 64 + ((lane >> 4) & 1) * 32 + (lane & 3) * 8;
    bf16x8 sel;
    { const short one = ((lane & 15) == ((lane >> 4) & 1)) ? (short)0x3f80 : (short)0;
#pragma unroll
      for (int j = 0; j < 8; ++j) sel[j] = one; }
    bf16x8 qf[2][2];
    float mref[2];
    f32x16 o[2]; f32x4 ls[2];
    for (int attempt = 0; attempt < 2; ++attempt) {
        if (wid < 4) { stage(0, 0); stage(1, 1); asm volatile("s_waitcnt vmcnt(8)" ::: "memory"); STAMP(2, 2); WAIT_BAR(8); } else LGKM_BAR();
        if (attempt == 0) {
#pragma unroll
            for (int a = 0; a < 2; ++a)
#pragma unroll
                for (int s = 0; s < 2; ++s) qf[a][s] = lds_b128(lds + AT_Q + (wq * 64 + a * 32 + r32) * 64 + (2 * s + hi) * 16);
            const bf16x8 k0 = lds_b128(lds + kr[0]), k1 = lds_b128(lds + kr[1]);
#pragma unroll
            for (int a = 0; a < 2; ++a) { f32x16 s = {}; s = MFMA32(k0, qf[a][0], s); s = MFMA32(k1, qf[a][1], s);
                float m = s[0];
#pragma unroll
                for (int r = 1; r < 16; ++r) m = fmaxf(m, s[r]);
                auto sw = __builtin_amdgcn_permlane32_swap(__float_as_uint(m), __float_as_uint(m), false, false);
                mref[a] = fmaxf(__uint_as_float(sw[0]), __uint_as_float(sw[1])); }
        }
        f32x16 nm[2];
#pragma unroll
        for (int a = 0; a < 2; ++a) {
#pragma unroll
            for (int r = 0; r < 16; ++r) nm[a][r] = -mref[a];
            o[a] = (f32x16){}; ls[a] = (f32x4){}; }
        bf16x8 kA[2], kB[2]; s16x4 vA[4], vB[4]; f32x16 sa_, sb_; u32x4 pa0, pa1, pb0, pb1;
        { const lds_ptr base = lds + kh * (2 * AT_KT * 64);
          kA[0] = lds_b128(base + kr[0]); kA[1] = lds_b128(base + kr[1]);
          sa_ = MFMA32(kA[0], qf[0][0], nm[0]); sa_ = MFMA32(kA[1], qf[0][1], sa_);
          pb0 = (u32x4){0u, 0u, 0u, 0u}; pb1 = pb0;
#pragma unroll
          for (int i = 0; i < 4; ++i) vB[i] = (s16x4){0, 0, 0, 0}; }
#define EXP2(S, i) S[i] = __builtin_amdgcn_exp2f(S[i])
#define PF(x) __builtin_bit_cast(bf16x8, x)
#define ASTEP(SC, PC0, PC1, SN, KF, QN, NMN, ON, LN_, VF, PP0, PP1, R0, R1, R2, R3, R4, R5) do { \
            SN = MFMA32(KF[0], QN[0], NMN); SB0(); EXP2(SC, 0); EXP2(SC, 1); EXP2(SC, 2); R0; SB0(); \
            SN = MFMA32(KF[1], QN[1], SN); SB0(); EXP2(SC, 3); EXP2(SC, 4); EXP2(SC, 5); PC0[0] = cvtpk(SC[0], SC[1]); R1; SB0(); \
            ON = MFMA32(cat4(VF[0], VF[1]), PF(PP0), ON); SB0(); EXP2(SC, 6); EXP2(SC, 7); EXP2(SC, 8); PC0[1] = cvtpk(SC[2], SC[3]); R2; SB0(); \
            ON = MFMA32(cat4(VF[2], VF[3]), PF(PP1), ON); SB0(); EXP2(SC, 9); EXP2(SC, 10); EXP2(SC, 11); PC0[2] = cvtpk(SC[4], SC[5]); R3; SB0(); \
            LN_ = MFMA16(sel, PF(PP0), LN_); SB0(); EXP2(SC, 12); EXP2(SC, 13); PC0[3] = cvtpk(SC[6], SC[7]); PC1[0] = cvtpk(SC[8], SC[9]); R4; SB0(); \
            LN_ = MFMA16(sel, PF(PP1), LN_); SB0(); EXP2(SC, 14); EXP2(SC, 15); PC1[1] = cvtpk(SC[10], SC[11]); PC1[2] = cvtpk(SC[12], SC[13]); R5; PC1[3] = cvtpk(SC[14], SC[15]); asm volatile("" : "+v"(PC0), "+v"(PC1)); SB0(); } while (0)
#define ATILE(KC, VP, KN, VN, kb, vb) do { \
            ASTEP(sa_, pa0, pa1, sb_, KC, qf[1], nm[1], o[1], ls[1], VP, pb0, pb1, KN[0] = lds_b128((kb) + kr[0]), KN[1] = lds_b128((kb) + kr[1]), \
                  VN[0] = lds_tr((vb) + vr), VN[1] = lds_tr((vb) + vr + 512), VN[2] = lds_tr((vb) + vr + 1024), VN[3] = lds_tr((vb) + vr + 1536)); \
            ASTEP(sb_, pb0, pb1, sa_, KN, qf[0], nm[0], o[0], ls[0], VN, pa0, pa1, (void)0, (void)0, (void)0, (void)0, (void)0, (void)0); } while (0)
        int slot = 0;
        STAMP(2, 3);
        for (int st = 0; st < AT_NST; ++st) {
            const int nslot = (slot == 2) ? 0 : slot + 1;
            const lds_ptr base = lds + slot * AT_STAGE + kh * (2 * AT_KT * 64), nbase = lds + nslot * AT_STAGE + kh * (2 * AT_KT * 64);
            SB0();
            ATILE(kA, vB, kB, vA, base + 2048, base);
            ATILE(kB, vA, kA, vB, base + 4096, base + 2048);
            ATILE(kA, vB, kB, vA, base + 6144, base + 4096);
            if (wid < 4) {
                if (attempt == 0 && st == 5 && wid == 0) poll_tags(flags, hwait + lane, T, 16);
                if (attempt == 0 && st == 4 && wid == 1 && blockIdx.x == 0) {
#pragma unroll
                    for (int i = 0; i < 4; ++i) poll_tags(flags, F_ST + 64 * i + lane, T, 64);
                    if (lane == 0) __hip_atomic_store(flags + F_E, T, RLX_AGENT); }
                WAIT_BAR(0);
                if (st + 2 < AT_NST) stage(st + 2, (slot == 0) ? 2 : slot - 1);
            } else { if (st == 2) WAIT_BAR(0); else LGKM_BAR(); }
            if (attempt == 0 && st == 2 && hpub >= 0) publish(flags, hpub, T);
            if (attempt == 0 && st == AT_NST - 2) g2_stage(lds, g2, 0, 0);
            SB0();
            ATILE(kB, vA, kA, vB, nbase, base + 6144);
            slot = nslot;
        }
        o[1] = MFMA32(cat4(vB[0], vB[1]), PF(pb0), o[1]); o[1] = MFMA32(cat4(vB[2], vB[3]), PF(pb1), o[1]);
        ls[1] = MFMA16(sel, PF(pb0), ls[1]); ls[1] = MFMA16(sel, PF(pb1), ls[1]);
#undef ATILE
#undef ASTEP
#undef EXP2
#undef PF
        LGKM_BAR();
        STAMP(2, 4);
        if (attempt == 1) break;
        bool bad = false;
#pragma unroll
        for (int a = 0; a < 2; ++a) { bad |= !(ls[a][0] < 1.0e18f) || !(ls[a][1] < 1.0e18f);
#pragma unroll
            for (int r = 0; r < 16; ++r) bad |= !(fabsf(o[a][r]) < 1.0e30f); }
        LAS unsigned* fl = (LAS unsigned*)(lds + AT_FL);
        if (lane == 0) fl[wid] = __any(bad) ? 1u : 0u;
        LGKM_BAR();
        const u32x4 f0 = *(const LAS u32x4*)fl, f1 = *(const LAS u32x4*)(fl + 4);
        if ((f0[0] | f0[1] | f0[2] | f0[3] | f1[0] | f1[1] | f1[2] | f1[3]) == 0u) break;
        float mx[2] = {-INFINITY, -INFINITY};
        for (int st = 0; st < AT_NST; ++st) {
            if (wid < 4) stage(st, 0);
            WAIT_BAR(0);
#pragma unroll
            for (int h2 = 0; h2 < 2; ++h2)
                for (int kt = 0; kt < AT_KT / 32; ++kt) {
                    const lds_ptr base = lds + h2 * (2 * AT_KT * 64) + kt * 2048;
                    const bf16x8 k0 = lds_b128(base + kr[0]), k1 = lds_b128(base + kr[1]);
#pragma unroll
                    for (int a = 0; a < 2; ++a) { f32x16 s = {}; s = MFMA32(k0, qf[a][0], s); s = MFMA32(k1, qf[a][1], s);
#pragma unroll
                        for (int r = 0; r < 16; ++r) mx[a] = fmaxf(mx[a], s[r]); }
                }
            LGKM_BAR();
        }
#pragma unroll
        for (int a = 0; a < 2; ++a) { auto sw = __builtin_amdgcn_permlane32_swap(__float_as_uint(mx[a]), __float_as_uint(mx[a]), false, false);
            mref[a] = fmaxf(__uint_as_float(sw[0]), __uint_as_float(sw[1])); }
    }
    g2_stage(lds, g2, 1, 1);
    LAS float* xo = (LAS float*)lds + wq * (2 * 17 * 64);
    if (kh == 1) {
#pragma unroll
        for (int a = 0; a < 2; ++a) {
#pragma unroll
            for (int r = 0; r < 16; ++r) xo[(a * 17 + r) * 64 + lane] = o[a][r];
            if (lane < 16) { xo[(a * 17 + 16) * 64 + lane] = ls[a][0]; xo[(a * 17 + 16) * 64 + 16 + lane] = ls[a][1]; } }
    } else {
#pragma unroll
        for (int a = 0; a < 2; ++a) if (lane < 16) { xo[(a * 17 + 16) * 64 + 32 + lane] = ls[a][0]; xo[(a * 17 + 16) * 64 + 48 + lane] = ls[a][1]; }
    }
    LGKM_BAR();
    if (kh == 0) {
        const rsrc_t rsa = mkrsrc(ao, S_ * LDAO * 2);
#pragma unroll
        for (int a = 0; a < 2; ++a) {
            const float l = xo[(a * 17 + 16) * 64 + r32] + xo[(a * 17 + 16) * 64 + 32 + r32];
            const float rl = 1.0f / l;
            unsigned pk[8];
#pragma unroll
            for (int r = 0; r < 8; ++r) pk[r] = cvtpk((o[a][2 * r] + xo[(a * 17 + 2 * r) * 64 + lane]) * rl, (o[a][2 * r + 1] + xo[(a * 17 + 2 * r + 1) * 64 + lane]) * rl);
            const int dst = (qb * 256 + wq * 64 + a * 32 + r32) * LDAO + DFF + head * DH;
#pragma unroll
            for (int k = 0; k < 4; k += 2) {
                auto r0 = __builtin_amdgcn_permlane32_swap(pk[2 * k], pk[2 * k + 2], false, false); auto r1 = __builtin_amdgcn_permlane32_swap(pk[2 * k + 1], pk[2 * k + 3], false, false);
                st_wt((u32x4){r0[0], r1[0], r0[1], r1[1]}, rsa, (unsigned)(dst + 8 * k + 8 * hi) * 2u);
            }
        }
    }
    LGKM_BAR();
    STAMP(2, 5);
}

__device__ __forceinline__ void gemm2_body(lds_ptr lds, const G2& g2, const bf16_t* __restrict__ x, bf16_t* __restrict__ out, float* part, unsigned* flags, const unsigned T, const int rb, const int cb, const int kh, const int opub) {
    constexpr int NKT = 36;
    const int tid = threadIdx.x, lane = tid & 63, wid = __builtin_amdgcn_readfirstlane(tid >> 6), kg = wid >> 2, wm = (wid >> 1) & 1, wn = wid & 1;
    const int fr = lane & 15, g = lane >> 4, q = fr >> 2, p = lane & 3;
    const int m0 = rb * 128, n0 = cb * 128;
    const int ar = (64 * wm + fr) * 128 + (((4 * kg + g) ^ (fr & 7)) * 16);
    int br[4];
    { const int swz = 2 * (q | ((g & 1) << 2));
#pragma unroll
      for (int j = 0; j < 4; ++j) br[j] = 16384 + (32 * kg + 8 * g + q) * 256 + (((2 * (4 * wn + j) + (p >> 1)) ^ swz) * 16) + (p & 1) * 8; }
    f32x4 acc[4][4];
#pragma unroll
    for (int i = 0; i < 4; ++i)
#pragma unroll
        for (int j = 0; j < 4; ++j) acc[i][j] = (f32x4){0.f, 0.f, 0.f, 0.f};
    bf16x8 A0[4], A1[4]; s16x4 B0[4][2], B1[4][2];
    const int wid4 = wid;
    auto st1 = [&](int t, int sl, int i) {
        const int gt = kh * 36 + t; const lds_ptr d = lds + g2_slot(sl);
        if (i < 2) bl_lds_dev(g2.rA, d + (16 * wid4 + 8 * i) * 128, g2.aoff, t * 128 + i * (8 * LDAO * 2));
        else { const bool w = gt < 56; bl_lds(w ? g2.rW2 : g2.rWo, d + 16384 + (8 * wid4 + 4 * (i - 2)) * 256, g2.boff[i - 2], (w ? gt : gt - 56) * (64 * D_ * 2)); } };
#define RDONE(AN, BN_, so, r) do { if ((r) < 4) AN[(r) & 3] = lds_b128(lds + (so) + ar + ((r) & 3) * 2048); \
        else if ((r) < 12) BN_[(((r) - 4) >> 1) & 3][(r) & 1] = lds_tr(lds + (so) + br[(((r) - 4) >> 1) & 3] + ((r) & 1) * 1024); } while (0)
#define STEP(AC, BC, AN, BN_, so, tl, sl) do { \
        unroll<0, 16>([&](auto ic) { constexpr int idx = decltype(ic)::value; constexpr int j = idx >> 2, i = idx & 3; \
            acc[i][j] = MFMA16(cat4(BC[j][0], BC[j][1]), AC[i], acc[i][j]); SB0(); \
            RDONE(AN, BN_, so, idx); \
            if ((idx & 3) == 2) st1(tl, sl, idx >> 2); \
            SB0(); }); } while (0)
    STAMP(3, 0);
    g2_stage(lds, g2, 2, 2); g2_stage(lds, g2, 3, 3);
    WAIT_BAR(8);
    publish(flags, opub, T);
    STAMP(3, 1);
    unroll<0, 12>([&](auto ic) { constexpr int idx = decltype(ic)::value; RDONE(A0, B0, g2_slot(0), idx); });
    WAIT_BAR(8);
    for (int t = 0; t < NKT; t += 2) {
        if (kh == 1 && t == 12 && wid == 0) poll_tags(flags, F_O + (rb >> 1) * 32 + (lane & 31), T, 32);
        SB0();
        STEP(A0, B0, A1, B1, g2_slot(t + 1), min(t + 4, NKT - 1), t);
        WAIT_BAR(8); SB0();
        STEP(A1, B1, A0, B0, g2_slot(t + 2), min(t + 5, NKT - 1), t + 1);
        WAIT_BAR(8);
    }
#undef RDONE
#undef STEP
    WAIT_BAR(0);
    STAMP(3, 2);
    LAS f32x4* xr = (LAS f32x4*)lds + (wid & 3) * 1024;
#pragma unroll
    for (int i = 0; i < 2; ++i)
#pragma unroll
        for (int j = 0; j < 4; ++j) xr[(kg * 8 + i * 4 + j) * 64 + lane] = kg ? acc[i][j] : acc[2 + i][j];
    LGKM_BAR();
    f32x4 r[2][4];
#pragma unroll
    for (int i = 0; i < 2; ++i)
#pragma unroll
        for (int j = 0; j < 4; ++j) r[i][j] = (kg ? acc[2 + i][j] : acc[i][j]) + xr[((kg ^ 1) * 8 + i * 4 + j) * 64 + lane];
    const int tile = rb * 8 + cb;
    { const rsrc_t rs = mkrsrc(part + (size_t)(tile * 2 + kh) * 8192, 32768);
#pragma unroll
      for (int j = 0; j < 4; ++j) st_wt(__builtin_bit_cast(u32x4, kh ? r[0][j] : r[1][j]), rs, (wid * 4 + j) * 1024 + lane * 16); }
    WAIT_BAR(0);
    publish(flags, F_P + (tile * 2 + kh) * 32, T);
    const int mrow = m0 + 64 * wm + 32 * kg + 16 * kh + fr;
    u32x2 xv[4];
#pragma unroll
    for (int j = 0; j < 4; ++j) xv[j] = *(const u32x2*)(x + (size_t)mrow * D_ + n0 + 64 * wn + 16 * j + 4 * g);
    if (wid == 0) poll_tags(flags, F_P + (tile * 2 + (kh ^ 1)) * 32, T, 1);
    __syncthreads();
    STAMP(3, 3);
    { const rsrc_t rs = mkrsrc(part + (size_t)(tile * 2 + (kh ^ 1)) * 8192, 32768);
      f32x4 o[4];
#pragma unroll
      for (int j = 0; j < 4; ++j) o[j] = __builtin_bit_cast(f32x4, __builtin_amdgcn_raw_buffer_load_b128(rs, (wid * 4 + j) * 1024 + lane * 16, 0, 16));
      unsigned pk[4][2];
#pragma unroll
      for (int j = 0; j < 4; ++j) { f32x4 v = (kh ? r[1][j] : r[0][j]) + o[j];
          v[0] += __uint_as_float(xv[j][0] << 16); v[1] += __uint_as_float(xv[j][0] & 0xffff0000u); v[2] += __uint_as_float(xv[j][1] << 16); v[3] += __uint_as_float(xv[j][1] & 0xffff0000u);
          pk[j][0] = cvtpk(v[0], v[1]); pk[j][1] = cvtpk(v[2], v[3]); }
#pragma unroll
      for (int j = 0; j < 4; j += 2) {
          auto r0 = __builtin_amdgcn_permlane16_swap(pk[j][0], pk[j + 1][0], false, false); auto r1 = __builtin_amdgcn_permlane16_swap(pk[j][1], pk[j + 1][1], false, false);
          *(u32x4*)(out + (size_t)mrow * D_ + n0 + 64 * wn + 16 * (j + (g & 1)) + 8 * (g >> 1)) = (u32x4){r0[0], r1[0], r0[1], r1[1]}; } }
    STAMP(3, 4);
}

__global__ __launch_bounds__(512, 2) void fused_kernel(const bf16_t* __restrict__ wqkv, const bf16_t* __restrict__ w1, const bf16_t* __restrict__ w2, const bf16_t* __restrict__ wo,
                                                       const bf16_t* __restrict__ x, bf16_t* qkv, bf16_t* ao, bf16_t* __restrict__ out, float* part, unsigned* flags) {
    __shared__ __attribute__((aligned(16))) char smem[LDS_BYTES];
    const lds_ptr lds = (lds_ptr)smem;
    const int xg = blockIdx.x & 7, r = blockIdx.x >> 3;
    const int rb1 = (xg & 1) * 4 + (r & 3), c1 = (xg >> 1) * 4 + ((r >> 2) & 3), ffn = r >> 4;
    unsigned T;
    if (ffn) T = gemm1_body<7>(lds, x, w1, DFF, rb1 * 256, c1 * 224, qkv, ao, flags, 16 + c1);
    else { T = gemm1_body<6>(lds, x, wqkv, 3 * D_, rb1 * 256, c1 * 192, qkv, ao, flags, c1); publish(flags, F_Q + c1 * 32 + rb1, T); }
    const int kh = xg & 1, rb3 = ((xg >> 1) & 1) * 8 + (r & 7), cb3 = (xg >> 2) * 4 + (r >> 3);
    const G2 g2 = g2_setup(ao, w2, wo, rb3, cb3, kh);
    const int head = xg * 4 + (r >> 3), qb = r & 7;
    attn_body(lds, qkv, ao, flags, T, head, qb, ffn ? F_H + rb1 * 32 + c1 : -1, F_H + (rb3 >> 1) * 32, g2);
    gemm2_body(lds, g2, x, out, part, flags, T, rb3, cb3, kh, F_O + qb * 32 + head);
}

extern "C" void kernel_launch(void* const* d_in, const int* in_sizes, int n_in, void* d_out, int out_size, void* d_ws, size_t ws_size, hipStream_t stream) {
    const bf16_t* x = (const bf16_t*)d_in[0]; const bf16_t* wqkv = (const bf16_t*)d_in[1]; const bf16_t* wo = (const bf16_t*)d_in[2];
    const bf16_t* w1 = (const bf16_t*)d_in[4]; const bf16_t* w2 = (const bf16_t*)d_in[5];
    char* ws = (char*)d_ws;
    unsigned* flags = (unsigned*)(ws + WS_FLAGS); bf16_t* qkv = (bf16_t*)(ws + WS_QKV); bf16_t* ao = (bf16_t*)(ws + WS_AO); float* part = (float*)(ws + WS_PART);
    fused_kernel<<<256, 512, 0, stream>>>(wqkv, w1, w2, wo, x, qkv, ao, (bf16_t*)d_out, part, flags);
}
```

```cpp
#include <hip/hip_runtime.h>
#include <hip/hip_bf16.h>
#include <stdint.h>

typedef unsigned short bf16_t;
typedef short bf16x8 __attribute__((ext_vector_type(8)));
typedef short s16x4 __attribute__((ext_vector_type(4)));
typedef float f32x2 __attribute__((ext_vector_type(2)));
typedef float f32x4 __attribute__((ext_vector_type(4)));
typedef float f32x16 __attribute__((ext_vector_type(16)));
typedef unsigned u32x4 __attribute__((ext_vector_type(4)));
typedef unsigned u32x2 __attribute__((ext_vector_type(2)));
typedef __bf16 bf16x2n __attribute__((ext_vector_type(2)));
typedef __bf16 bf16x4n __attribute__((ext_vector_type(4)));
#define LAS __attribute__((address_space(3)))
typedef LAS char* lds_ptr;

constexpr int S_ = 2048, D_ = 1024, H_ = 32, DH = 32, DFF = 3584;
constexpr int LDAO = DFF + D_;
constexpr size_t WS_FLAGS = 0;
constexpr size_t WS_QKV = 1u << 20;
constexpr size_t WS_AO = WS_QKV + (size_t)3 * S_ * D_ * 2;
constexpr size_t WS_PART = WS_AO + (size_t)S_ * LDAO * 2;
constexpr int LDS_BYTES = 163840;
constexpr int F_E = 0, F_ST = 256, F_Q = 1024, F_H = 2048, F_O = 3072, F_P = 4096;
constexpr int F_LT = 512, F_CT = 768;
constexpr int F_LN = 16384, F_CS = 32768;
constexpr int NCOL = 3 * D_ + DFF;
constexpr int TBL = 151552;

#define EXP_NOADD 0
#define EXP_NOROT 0
#define EXP_NOHST 0
#define STAMP(k, i) do {} while (0)

__device__ __forceinline__ unsigned cvtpk(float lo, float hi) { return __builtin_bit_cast(unsigned, __builtin_convertvector((f32x2){lo, hi}, bf16x2n)); }
__device__ __forceinline__ float rbf(float x) { return __uint_as_float(cvtpk(x, x) << 16); }
typedef __amdgpu_buffer_rsrc_t rsrc_t;
__device__ __forceinline__ rsrc_t mkrsrc(const void* p, unsigned bytes) { return __builtin_amdgcn_make_buffer_rsrc((void*)p, 0, bytes, 0x00020000); }
__device__ __forceinline__ void bl_lds(rsrc_t r, lds_ptr l, unsigned voff, unsigned soff) {
    asm volatile("s_mov_b32 m0, %0\n\ts_nop 0\n\tbuffer_load_dwordx4 %1, %2, %3 offen lds" :: "s"(__builtin_amdgcn_readfirstlane((unsigned)(uintptr_t)l)), "v"(voff), "s"(r), "s"(__builtin_amdgcn_readfirstlane(soff)) : "memory"); }
__device__ __forceinline__ void bl_lds_dev(rsrc_t r, lds_ptr l, unsigned voff, unsigned soff) {
    asm volatile("s_mov_b32 m0, %0\n\ts_nop 0\n\tbuffer_load_dwordx4 %1, %2, %3 offen sc1 lds" :: "s"(__builtin_amdgcn_readfirstlane((unsigned)(uintptr_t)l)), "v"(voff), "s"(r), "s"(__builtin_amdgcn_readfirstlane(soff)) : "memory"); }
__device__ __forceinline__ void st_wt(u32x4 v, rsrc_t r, unsigned off) { __builtin_amdgcn_raw_buffer_store_b128(v, r, off, 0, 16); }
__device__ __forceinline__ bf16x8 lds_b128(lds_ptr p) { return *(const LAS bf16x8*)p; }
__device__ __forceinline__ s16x4 lds_tr(lds_ptr p) { return __builtin_amdgcn_ds_read_tr16_b64_v4i16((LAS s16x4*)p); }
__device__ __forceinline__ bf16x8 cat4(s16x4 a, s16x4 b) { return (bf16x8){a[0], a[1], a[2], a[3], b[0], b[1], b[2], b[3]}; }
#define MFMA16(a, b, c) __builtin_amdgcn_mfma_f32_16x16x32_bf16(a, b, c, 0, 0, 0)
#define MFMA32(a, b, c) __builtin_amdgcn_mfma_f32_32x32x16_bf16(a, b, c, 0, 0, 0)
#define WAIT_BAR(N) asm volatile("s_waitcnt vmcnt(%0) lgkmcnt(0)\n\ts_barrier" :: "n"(N) : "memory")
#define LGKM_BAR() asm volatile("s_waitcnt lgkmcnt(0)\n\ts_barrier" ::: "memory")
#define SB0() __builtin_amdgcn_sched_barrier(0)
#define RLX_AGENT __ATOMIC_RELAXED, __HIP_MEMORY_SCOPE_AGENT
template <int I> struct IC { static constexpr int value = I; };
template <int B, int E, class F> __device__ __forceinline__ void unroll(F&& f) { if constexpr (B < E) { f(IC<B>{}); unroll<B + 1, E>(f); } }

__device__ __forceinline__ float wave_sum(float v) {
    v += __builtin_bit_cast(float, __builtin_amdgcn_update_dpp(0, __builtin_bit_cast(int, v), 0xB1, 0xf, 0xf, true));
    v += __builtin_bit_cast(float, __builtin_amdgcn_update_dpp(0, __builtin_bit_cast(int, v), 0x4E, 0xf, 0xf, true));
    v += __builtin_bit_cast(float, __builtin_amdgcn_update_dpp(0, __builtin_bit_cast(int, v), 0x141, 0xf, 0xf, true));
    v += __builtin_bit_cast(float, __builtin_amdgcn_update_dpp(0, __builtin_bit_cast(int, v), 0x140, 0xf, 0xf, true));
    { auto r = __builtin_amdgcn_permlane16_swap(__float_as_uint(v), __float_as_uint(v), false, false); v = __uint_as_float(r[0]) + __uint_as_float(r[1]); }
    { auto r = __builtin_amdgcn_permlane32_swap(__float_as_uint(v), __float_as_uint(v), false, false); v = __uint_as_float(r[0]) + __uint_as_float(r[1]); }
    return v;
}
__device__ __forceinline__ void poll_tags(unsigned* flags, int word, unsigned T, int n) {
    const bool act = (int)(threadIdx.x & 63) < n; unsigned spins = 0;
    for (;;) { unsigned v = T; if (act) v = __hip_atomic_load(flags + word, RLX_AGENT);
        if (__all(v == T)) break; __builtin_amdgcn_s_sleep(2); if (++spins > (1u << 22)) break; }
    asm volatile("" ::: "memory");
}
__device__ __forceinline__ void publish(unsigned* flags, int word, unsigned T) {
    if (threadIdx.x == 0) __hip_atomic_store(flags + word, T, RLX_AGENT);
}
typedef unsigned long long u64;
__device__ __forceinline__ void put_granule(unsigned* flags, int word, float v, unsigned T) { __hip_atomic_store((u64*)(flags + word), ((u64)T << 32) | __float_as_uint(v), RLX_AGENT); }
__device__ __forceinline__ u64 get_granule(unsigned* flags, int word) { return __hip_atomic_load((u64*)(flags + word), RLX_AGENT); }

constexpr int JUNK = 162816;
constexpr int HSTG = 65536;
template <int NT>
__device__ __forceinline__ unsigned gemm1_body(lds_ptr lds, const bf16_t* xn, const bf16_t* W, const int ldw, const int m0, const int n0, bf16_t* qkv, bf16_t* ao, unsigned* flags, const int j32, const bf16_t* wq_, const bf16_t* w1_) {
    constexpr int BN = 32 * NT, RB = BN * 2, CPR = BN / 8, BSLOT = 64 * RB, NKT = D_ / 64, GB = (NT == 7) ? 4 : 3;
    constexpr int LB = 65536, NMF = 4 * NT, NRD = 4 + 2 * NT, NBB = (NT == 7) ? 2 : 4;
    const int tid = threadIdx.x, lane = tid & 63, wid = __builtin_amdgcn_readfirstlane(tid >> 6), wm = wid & 3, wn = wid >> 2;
    const int fr = lane & 15, g = lane >> 4, q = fr >> 2, p = lane & 3;
    const rsrc_t rA = mkrsrc(xn + (size_t)m0 * D_, 256 * D_ * 2), rB = mkrsrc(W + n0, (unsigned)(D_ * ldw - n0) * 2u);
    const unsigned aoff = (unsigned)((32 * wid + (lane >> 3)) * D_ + (((lane & 7) ^ ((lane >> 3) & 7)) * 8)) * 2u;
    unsigned boff[GB];
#pragma unroll
    for (int i = 0; i < GB; ++i) { const int idx = 64 * i + lane, rl = idx / CPR, cp = idx % CPR, k = (8 * wid + rl) & 63;
        const int swz = (NT == 7) ? 2 * ((k >> 3) & 1) : 2 * (((k >> 1) & 1) | (((k >> 3) & 1) << 1));
        boff[i] = (unsigned)(k * ldw + (cp ^ swz) * 8) * 2u; }
    const int krot = (m0 >> 10) * 8, tcs = ((m0 >> 8) & 3) * 2;
    auto stageA1 = [&](int kt_, int slot, int i) { const int kt = (kt_ + krot) & (NKT - 1); bl_lds(rA, lds + slot * 32768 + (32 * wid + 8 * i) * 128, aoff, kt * 128 + i * (8 * D_ * 2)); };
    auto stageB1 = [&](int kt_, int slot, int i) { const int kt = (kt_ + krot) & (NKT - 1); const unsigned so = (unsigned)(kt * 64 * ldw) * 2u;
        if (NT == 7 && i == 3) { if (lane < 32) bl_lds(rB, lds + LB + slot * BSLOT + (8 * wid * CPR + 192) * 16, boff[GB - 1], so); }
        else bl_lds(rB, lds + LB + slot * BSLOT + (8 * wid * CPR + 64 * i) * 16, boff[i < GB ? i : 0], so); };
    int ar[2];
#pragma unroll
    for (int ks = 0; ks < 2; ++ks) ar[ks] = (64 * wm + fr) * 128 + (((4 * ks + g) ^ (fr & 7)) * 16);
    int br[NBB];
    { const int swz = (NT == 7) ? 2 * (g & 1) : 2 * ((q >> 1) | ((g & 1) << 1));
#pragma unroll
      for (int j = 0; j < NBB; ++j) br[j] = LB + (8 * g + q) * RB + (((2 * (NT * wn + j) + (p >> 1)) ^ swz) * 16) + (p & 1) * 8; }
    f32x4 acc[4][NT];
#pragma unroll
    for (int i = 0; i < 4; ++i)
#pragma unroll
        for (int j = 0; j < NT; ++j) acc[i][j] = (f32x4){0.f, 0.f, 0.f, 0.f};
    bf16x8 A0[4], A1[4]; s16x4 B0[NT][2], B1[NT][2];
#define RDONE(AN, BN_, sa, sb, ks, r) do { if ((r) < 4) AN[(r) < 4 ? (r) : 0] = lds_b128(lds + (sa) + ar[ks] + ((r) < 4 ? (r) : 0) * 2048); \
        else if ((r) < NRD) { constexpr int jj = (((r) - 4) >> 1) < NT && (r) >= 4 ? (((r) - 4) >> 1) : 0, tt = ((r) - 4) & 1; BN_[jj][tt] = lds_tr(lds + (sb) + br[jj % NBB] + 32 * (jj - jj % NBB) + (32 * (ks) + 4 * tt) * RB); } } while (0)
#define SUBSTEP(AC, BC, AN, BN_, sa, sb, ks, STG, go, tl, sl) do { \
        unroll<0, NMF>([&](auto ic) { constexpr int idx = decltype(ic)::value; constexpr int j = idx >> 2, i = idx & 3; \
            acc[i][j] = MFMA16(cat4(BC[j][0], BC[j][1]), AC[i], acc[i][j]); SB0(); \
            RDONE(AN, BN_, sa, sb, ks, idx); \
            if ((idx % NT) == 1 && (go)) { if (STG == 1) stageA1(tl, sl, idx / NT); if (STG == 2 && idx / NT < GB) stageB1(tl, sl, idx / NT); } \
            SB0(); }); } while (0)
    STAMP(1, 0);
#pragma unroll
    for (int t = 0; t < 2; ++t) {
#pragma unroll
        for (int i = 0; i < GB; ++i) stageB1(t, t, i);
#pragma unroll
        for (int i = 0; i < 4; ++i) stageA1(t, t, i); }
    unsigned T; float lnv;
    const int ncg = (NT == 7 ? 3 * D_ : 0) + n0;
    {   const int row = m0 + 8 * j32 + wid;
        const u32x4* src = (const u32x4*)(xn + (size_t)row * D_);
        u32x4 v0, v1; unsigned ev;
        asm volatile("global_load_dwordx4 %0, %3, off\n\tglobal_load_dwordx4 %1, %3, off offset:1024\n\tglobal_load_dword %2, %4, off sc1"
                     : "=&v"(v0), "=&v"(v1), "=&v"(ev) : "v"(src + lane), "v"(flags + F_E) : "memory");
        asm volatile("s_waitcnt vmcnt(0)" : "+v"(v0), "+v"(v1), "+v"(ev) :: "memory");
        SB0();
        float f[16];
#pragma unroll
        for (int i = 0; i < 4; ++i) { f[2 * i] = __uint_as_float(v0[i] << 16); f[2 * i + 1] = __uint_as_float(v0[i] & 0xffff0000u); f[8 + 2 * i] = __uint_as_float(v1[i] << 16); f[8 + 2 * i + 1] = __uint_as_float(v1[i] & 0xffff0000u); }
        float sm = 0.f;
#pragma unroll
        for (int i = 0; i < 16; ++i) sm += f[i];
        const float mean = wave_sum(sm) * (1.0f / D_);
        float qq = 0.f;
#pragma unroll
        for (int i = 0; i < 16; ++i) { const float d = f[i] - mean; qq += d * d; }
        qq = wave_sum(qq);
        T = (unsigned)__builtin_amdgcn_readfirstlane(ev) + 1u;
        const float sd = rbf(__fsqrt_rn(rbf(rbf(qq * (1.0f / D_)) + 1e-5f)));
        lnv = (lane & 1) ? 1.0f / sd : rbf(mean);
    }
    WAIT_BAR(0);
    auto colsum = [&](s16x4 (&BC)[NT][2]) {
        const bf16x8 ones = {0x3f80, 0x3f80, 0x3f80, 0x3f80, 0x3f80, 0x3f80, 0x3f80, 0x3f80}; const f32x4 z = {0.f, 0.f, 0.f, 0.f};
        float c[NT];
#pragma unroll
        for (int j = 0; j < NT; ++j) c[j] = MFMA16(ones, cat4(BC[j][0], BC[j][1]), z)[0];
        if (lane < 16) {
#pragma unroll
            for (int j = 0; j < NT; ++j) ((LAS float*)(lds + TBL + 2048))[wm * 256 + 16 * (NT * wn + j) + lane] = c[j]; } };
    STAMP(1, 1);
    unroll<0, NRD>([&](auto ic) { constexpr int idx = decltype(ic)::value; RDONE(A0, B0, 0, 0, 0, idx); });
    int sa = 0, sb = 0, sb3 = 0;
    const rsrc_t rF = mkrsrc(flags, 1u << 20);
    unsigned long long itv = 0;
    const unsigned evmask = 1u | (1u << (tcs + (wm >> 1))) | (1u << (tcs + 3)) | (1u << (NKT - 2)) | (wid == 0 ? (4u | (1u << (tcs + 5)) | (1u << (NKT - 3))) : 0u);
    for (int t = 0; t < NKT; ++t) {
        const int tn = min(t + 2, NKT - 1); constexpr bool go = true;
        SB0();
        { const int slb = (sb3 == 0) ? 2 : sb3 - 1;
          SUBSTEP(A0, B0, A1, B1, sa, sb, 1, 2, go, tn, slb); }
        if (__builtin_expect((evmask >> t) & 1u, 0)) {
            if (t == 0) { if (lane < 2) __hip_atomic_store(flags + F_LN + (m0 + 8 * j32 + wid) * 2 + lane, __float_as_uint(lnv), RLX_AGENT);
                if (wid == 0) { publish(flags, F_ST + blockIdx.x, T); WAIT_BAR(GB + 2); } else WAIT_BAR(GB + 1); }
            else if (wid == 0 && (t == 2 || t == tcs + 5)) { publish(flags, (t == 2 ? F_LT : F_CT) + (m0 >> 8) * 32 + j32, T); WAIT_BAR(GB + 1); }
            else if (wid * 64 < BN && t == tcs + 3) { if (tid < BN) { const LAS float* pc = (const LAS float*)(lds + TBL + 2048) + tid;
                    __hip_atomic_store(flags + F_CS + (m0 >> 8) * NCOL + ncg + tid, __float_as_uint((pc[0] + pc[256]) + (pc[512] + pc[768])), RLX_AGENT); }
                WAIT_BAR(GB + 1); }
            else WAIT_BAR(GB);
            if (t == tcs + (wm >> 1)) { if (wm & 1) colsum(B1); else colsum(B0); }
            if (t == NKT - 3 && wid == 0) poll_tags(flags, lane < 32 ? F_LT + (m0 >> 8) * 32 + lane : F_CT + (lane & 7) * 32 + j32, T, 40);
            if (t == NKT - 2) {
                if (lane < BN / 4) bl_lds_dev(rF, lds + TBL + 2048 + wid * 1024, lane * 16, (F_CS + wid * NCOL + ncg) * 4);
                if (wid < 2) bl_lds_dev(rF, lds + TBL + wid * 1024, lane * 16, (F_LN + m0 * 2) * 4 + wid * 1024); }
        } else WAIT_BAR(GB);
        SB0();
        sa ^= 32768; sb3 = (sb3 == 2) ? 0 : sb3 + 1; sb = sb3 * BSLOT;
        SUBSTEP(A1, B1, A0, B0, sa, sb, 0, 1, go, tn, t & 1);
    }
#undef RDONE
#undef SUBSTEP
    STAMP(1, 2);
    if (tid < BN) { float cs = 0.f;
#pragma unroll
        for (int i = 0; i < 8; ++i) cs += ((const LAS float*)(lds + TBL + 2048))[i * 256 + tid];
        ((LAS float*)(lds + TBL + 10240))[tid] = cs; }
    WAIT_BAR(0);
#pragma unroll
    for (int i = 0; i < 4; ++i) { const f32x2 st = ((const LAS f32x2*)(lds + TBL))[64 * wm + 16 * i + fr];
#pragma unroll
        for (int j = 0; j < NT; ++j) { const f32x4 cs = *(const LAS f32x4*)(lds + TBL + 10240 + (16 * (NT * wn + j) + 4 * g) * 4); acc[i][j] = (acc[i][j] - cs * st[0]) * st[1]; } }
    const int mrow = m0 + 64 * wm + fr;
    if constexpr (NT == 6) {
        const rsrc_t rsq = mkrsrc(qkv, 3 * S_ * D_ * 2);
#pragma unroll
        for (int b = 0; b < 3; ++b) {
            const int col = n0 + 96 * wn + 32 * b, which = col >> 10, head = (col & 1023) >> 5;
            const float sc = (which == 0) ? 0.17677669529663687f * 1.4426950408889634f : 1.0f;
            const int dst = ((which * H_ + head) * S_) * DH;
#pragma unroll
            for (int i = 0; i < 4; ++i) {
                const f32x4 u = acc[i][2 * b] * sc, v = acc[i][2 * b + 1] * sc;
                unsigned x0 = cvtpk(u[0], u[1]), x1 = cvtpk(u[2], u[3]), y0 = cvtpk(v[0], v[1]), y1 = cvtpk(v[2], v[3]);
                auto r0 = __builtin_amdgcn_permlane16_swap(x0, y0, false, false); auto r1 = __builtin_amdgcn_permlane16_swap(x1, y1, false, false);
                const int tok = mrow + 16 * i; int ch = 2 * (g & 1) + (g >> 1); if (which == 1) ch ^= (tok >> 2) & 3;
                st_wt((u32x4){r0[0], r1[0], r0[1], r1[1]}, rsq, (unsigned)(dst + tok * DH + ch * 8) * 2u);
            }
        }
        WAIT_BAR(0);
    } else {
        const rsrc_t rsa = mkrsrc(ao, S_ * LDAO * 2);
        u32x4 sv[14];
        auto pk = [&](const f32x4& a, const f32x4& b) { const f32x4 z = {0.f, 0.f, 0.f, 0.f}; const f32x4 u = __builtin_elementwise_max(a, z), v = __builtin_elementwise_max(b, z);
            unsigned x0 = cvtpk(u[0], u[1]), x1 = cvtpk(u[2], u[3]), y0 = cvtpk(v[0], v[1]), y1 = cvtpk(v[2], v[3]);
            auto r0 = __builtin_amdgcn_permlane16_swap(x0, y0, false, false); auto r1 = __builtin_amdgcn_permlane16_swap(x1, y1, false, false); return (u32x4){r0[0], r1[0], r0[1], r1[1]}; };
        auto off = [&](int s, int half) -> unsigned {
            const int c0 = n0 + 112 * half;
            if (s < 12) return (unsigned)((mrow + 16 * (s & 3)) * LDAO + c0 + 16 * half + 32 * (s >> 2) + 16 * (g & 1) + 8 * (g >> 1)) * 2u;
            return (unsigned)((mrow + 16 * (2 * (s - 12) + (g & 1))) * LDAO + c0 + (half ? 0 : 96) + 8 * (g >> 1)) * 2u; };
        if (wn == 0) {
#pragma unroll
            for (int b = 0; b < 3; ++b)
#pragma unroll
                for (int i = 0; i < 4; ++i) sv[4 * b + i] = pk(acc[i][2 * b], acc[i][2 * b + 1]);
#pragma unroll
            for (int i = 0; i < 2; ++i) sv[12 + i] = pk(acc[2 * i][6], acc[2 * i + 1][6]);
#pragma unroll
            for (int s = 0; s < 14; ++s) *(LAS u32x4*)(lds + HSTG + (wm * 14 + s) * 1024 + lane * 16) = sv[s];
            LGKM_BAR();
        } else {
#pragma unroll
            for (int b = 0; b < 3; ++b)
#pragma unroll
                for (int i = 0; i < 4; ++i) sv[4 * b + i] = pk(acc[i][2 * b + 1], acc[i][2 * b + 2]);
#pragma unroll
            for (int i = 0; i < 2; ++i) sv[12 + i] = pk(acc[2 * i][0], acc[2 * i + 1][0]);
#pragma unroll
            for (int s = 0; s < 14; ++s) if (!EXP_NOHST) st_wt(sv[s], rsa, off(s, 1));
            LGKM_BAR();
#pragma unroll
            for (int s = 0; s < 14; ++s) sv[s] = *(const LAS u32x4*)(lds + HSTG + (wm * 14 + s) * 1024 + lane * 16);
#pragma unroll
            for (int s = 0; s < 14; ++s) if (!EXP_NOHST || sv[s][0] == 0x12345678) st_wt(sv[s], rsa, off(s, 0));
        }
    }
    STAMP(1, 3);
    return T;
}

struct G2 { const bf16_t *ao_, *w2_, *wo_; unsigned aoff, boff[2]; int rb, cb, kh;
    __device__ __forceinline__ rsrc_t rA() const { return mkrsrc(ao_ + (size_t)rb * 128 * LDAO + kh * 2304, (unsigned)(128 * LDAO - kh * 2304) * 2u); }
    __device__ __forceinline__ rsrc_t rW2() const { return mkrsrc(w2_ + cb * 128, (unsigned)(DFF * D_ - cb * 128) * 2u); }
    __device__ __forceinline__ rsrc_t rWo() const { return mkrsrc(wo_ + cb * 128, (unsigned)(D_ * D_ - cb * 128) * 2u); } };
__device__ __forceinline__ G2 g2_setup(const bf16_t* ao, const bf16_t* w2, const bf16_t* wo, int rb, int cb, int kh) {
    const int lane = threadIdx.x & 63, wid = __builtin_amdgcn_readfirstlane(threadIdx.x >> 6);
    G2 s; s.kh = kh; s.rb = rb; s.cb = cb; s.ao_ = ao; s.w2_ = w2; s.wo_ = wo;
    s.aoff = (unsigned)((16 * wid + (lane >> 3)) * LDAO + (((lane & 7) ^ ((lane >> 3) & 7)) * 8)) * 2u;
#pragma unroll
    for (int i = 0; i < 2; ++i) { const int k = 8 * wid + 4 * i + (lane >> 4), cp = lane & 15, swz = 2 * ((k & 3) | (((k >> 3) & 1) << 2)); s.boff[i] = (unsigned)(k * D_ + (cp ^ swz) * 8) * 2u; }
    return s;
}
__device__ __forceinline__ int g2_slot(int t) { return (int)((0x10002030u >> (8 * (t & 3))) & 0xffu) * 2048; }
__device__ __forceinline__ void g2_stage(lds_ptr lds, const G2& s, int t, int sl) {
    const int wid = __builtin_amdgcn_readfirstlane(threadIdx.x >> 6), gt = s.kh * 36 + t; const lds_ptr d = lds + g2_slot(sl);
#pragma unroll
    for (int i = 0; i < 2; ++i) bl_lds_dev(s.rA(), d + (16 * wid + 8 * i) * 128, s.aoff, t * 128 + i * (8 * LDAO * 2));
    if (gt < 56) {
#pragma unroll
        for (int i = 0; i < 2; ++i) bl_lds(s.rW2(), d + 16384 + (8 * wid + 4 * i) * 256, s.boff[i], gt * (64 * D_ * 2));
    } else {
#pragma unroll
        for (int i = 0; i < 2; ++i) bl_lds(s.rWo(), d + 16384 + (8 * wid + 4 * i) * 256, s.boff[i], (gt - 56) * (64 * D_ * 2));
    }
}

constexpr int AT_KT = 128, AT_STAGE = 4 * AT_KT * 64, AT_NST = 1024 / AT_KT, AT_Q = 122880, AT_FL = 139264;
__device__ __forceinline__ void attn_body(lds_ptr lds, const bf16_t* qkv, bf16_t* ao, unsigned* flags, const unsigned T, const int head, const int qb, const int hpub, const int hwait, const G2& g2) {
    const int tid = threadIdx.x, lane = tid & 63, wid = __builtin_amdgcn_readfirstlane(tid >> 6), wq = wid & 3, kh = wid >> 2;
    const int r32 = lane & 31, hi = lane >> 5;
    const rsrc_t rs = mkrsrc(qkv, 3 * S_ * D_ * 2);
    const unsigned ssrc = (unsigned)((((1 + (wid & 1)) * H_ + head) * S_ + (wid >> 1) * 1024) * 64);
    auto stage = [&](int st, int slot) {
#pragma unroll
        for (int i = 0; i < 8; ++i) bl_lds_dev(rs, lds + slot * AT_STAGE + (wid & 3) * 8192 + i * 1024, lane * 16, ssrc + st * (AT_KT * 64) + i * 1024); };
    STAMP(2, 0);
    if (wid < 4) {
        poll_tags(flags, F_Q + (((lane >> 3) & 3) * 1024 + head * 32) / 192 * 32 + (lane & 7), T, 24);
        STAMP(2, 1);
#pragma unroll
        for (int i = 0; i < 4; ++i) bl_lds_dev(rs, lds + AT_Q + wid * 4096 + i * 1024, lane * 16, (unsigned)((head * S_ + qb * 256) * 64 + wid * 4096 + i * 1024));
    }
    const int kr[2] = {r32 * 64 + ((hi ^ ((r32 >> 2) & 3)) * 16), r32 * 64 + (((2 + hi) ^ ((r32 >> 2) & 3)) * 16)};
    const int vr = AT_KT * 64 + (4 * hi + ((lane & 15) >> 2)) * 64 + ((lane >> 4) & 1) * 32 + (lane & 3) * 8;
    bf16x8 sel;
    { const short one = ((lane & 15) == ((lane >> 4) & 1)) ? (short)0x3f80 : (short)0;
#pragma unroll
      for (int j = 0; j < 8; ++j) sel[j] = one; }
    bf16x8 qf[2][2];
    float mref[2];
    f32x16 o[2]; f32x4 ls[2];
    for (int attempt = 0; attempt < 2; ++attempt) {
        if (wid < 4) { stage(0, 0); stage(1, 1); asm volatile("s_waitcnt vmcnt(8)" ::: "memory"); STAMP(2, 2); WAIT_BAR(8); } else LGKM_BAR();
        if (attempt == 0) {
#pragma unroll
            for (int a = 0; a < 2; ++a)
#pragma unroll
                for (int s = 0; s < 2; ++s) qf[a][s] = lds_b128(lds + AT_Q + (wq * 64 + a * 32 + r32) * 64 + (2 * s + hi) * 16);
            const bf16x8 k0 = lds_b128(lds + kr[0]), k1 = lds_b128(lds + kr[1]);
#pragma unroll
            for (int a = 0; a < 2; ++a) { f32x16 s = {}; s = MFMA32(k0, qf[a][0], s); s = MFMA32(k1, qf[a][1], s);
                float m = s[0];
#pragma unroll
                for (int r = 1; r < 16; ++r) m = fmaxf(m, s[r]);
                auto sw = __builtin_amdgcn_permlane32_swap(__float_as_uint(m), __float_as_uint(m), false, false);
                mref[a] = fmaxf(__uint_as_float(sw[0]), __uint_as_float(sw[1])); }
        }
        f32x16 nm[2];
#pragma unroll
        for (int a = 0; a < 2; ++a) {
#pragma unroll
            for (int r = 0; r < 16; ++r) nm[a][r] = -mref[a];
            o[a] = (f32x16){}; ls[a] = (f32x4){}; }
        bf16x8 kA[2], kB[2]; s16x4 vA[4], vB[4]; f32x16 sa_, sb_; u32x4 pa0, pa1, pb0, pb1;
        { const lds_ptr base = lds + kh * (2 * AT_KT * 64);
          kA[0] = lds_b128(base + kr[0]); kA[1] = lds_b128(base + kr[1]);
          sa_ = MFMA32(kA[0], qf[0][0], nm[0]); sa_ = MFMA32(kA[1], qf[0][1], sa_);
          pb0 = (u32x4){0u, 0u, 0u, 0u}; pb1 = pb0;
#pragma unroll
          for (int i = 0; i < 4; ++i) vB[i] = (s16x4){0, 0, 0, 0}; }
#define EXP2(S, i) S[i] = __builtin_amdgcn_exp2f(S[i])
#define PF(x) __builtin_bit_cast(bf16x8, x)
#define ASTEP(SC, PC0, PC1, SN, KF, QN, NMN, ON, LN_, VF, PP0, PP1, R0, R1, R2, R3, R4, R5) do { \
            SN = MFMA32(KF[0], QN[0], NMN); SB0(); EXP2(SC, 0); EXP2(SC, 1); EXP2(SC, 2); R0; SB0(); \
            SN = MFMA32(KF[1], QN[1], SN); SB0(); EXP2(SC, 3); EXP2(SC, 4); EXP2(SC, 5); PC0[0] = cvtpk(SC[0], SC[1]); R1; SB0(); \
            ON = MFMA32(cat4(VF[0], VF[1]), PF(PP0), ON); SB0(); EXP2(SC, 6); EXP2(SC, 7); EXP2(SC, 8); PC0[1] = cvtpk(SC[2], SC[3]); R2; SB0(); \
            ON = MFMA32(cat4(VF[2], VF[3]), PF(PP1), ON); SB0(); EXP2(SC, 9); EXP2(SC, 10); EXP2(SC, 11); PC0[2] = cvtpk(SC[4], SC[5]); R3; SB0(); \
            LN_ = MFMA16(sel, PF(PP0), LN_); SB0(); EXP2(SC, 12); EXP2(SC, 13); PC0[3] = cvtpk(SC[6], SC[7]); PC1[0] = cvtpk(SC[8], SC[9]); R4; SB0(); \
            LN_ = MFMA16(sel, PF(PP1), LN_); SB0(); EXP2(SC, 14); EXP2(SC, 15); PC1[1] = cvtpk(SC[10], SC[11]); PC1[2] = cvtpk(SC[12], SC[13]); R5; PC1[3] = cvtpk(SC[14], SC[15]); asm volatile("" : "+v"(PC0), "+v"(PC1)); SB0(); } while (0)
#define ATILE(KC, VP, KN, VN, kb, vb) do { \
            ASTEP(sa_, pa0, pa1, sb_, KC, qf[1], nm[1], o[1], ls[1], VP, pb0, pb1, KN[0] = lds_b128((kb) + kr[0]), KN[1] = lds_b128((kb) + kr[1]), \
                  VN[0] = lds_tr((vb) + vr), VN[1] = lds_tr((vb) + vr + 512), VN[2] = lds_tr((vb) + vr + 1024), VN[3] = lds_tr((vb) + vr + 1536)); \
            ASTEP(sb_, pb0, pb1, sa_, KN, qf[0], nm[0], o[0], ls[0], VN, pa0, pa1, (void)0, (void)0, (void)0, (void)0, (void)0, (void)0); } while (0)
        const unsigned aev = wid >= 4 ? 0x54u : (0xc0u | (wid == 0 ? 0x24u : 0u) | ((wid == 1 && blockIdx.x == 0) ? 0x10u : 0u));
        int slot = 0;
        STAMP(2, 3);
        for (int st = 0; st < AT_NST; ++st) {
            const int nslot = (slot == 2) ? 0 : slot + 1;
            const lds_ptr base = lds + slot * AT_STAGE + kh * (2 * AT_KT * 64), nbase = lds + nslot * AT_STAGE + kh * (2 * AT_KT * 64);
            SB0();
            ATILE(kA, vB, kB, vA, base + 2048, base);
            ATILE(kB, vA, kA, vB, base + 4096, base + 2048);
            ATILE(kA, vB, kB, vA, base + 6144, base + 4096);
            if (__builtin_expect((aev >> st) & 1u, 0)) {
                if (wid < 4) {
                    if (attempt == 0 && st == 5 && wid == 0) poll_tags(flags, hwait + lane, T, 16);
                    if (attempt == 0 && st == 4) {
#pragma unroll 1
                        for (int i = 0; i < 4; ++i) poll_tags(flags, F_ST + 64 * i + lane, T, 64);
                        if (lane == 0) __hip_atomic_store(flags + F_E, T, RLX_AGENT); }
                    WAIT_BAR(0);
                    if (st + 2 < AT_NST) stage(st + 2, (slot == 0) ? 2 : slot - 1);
                } else {
                    WAIT_BAR(0);
                    if (attempt == 0 && st < 6) {
                        const int w = (blockIdx.x >> 3) * 4 + wid - 4; const bool second = st == 4;
                        const int n = g2.kh ? (second ? 8 : 10) : 9, k0 = g2.kh ? (second ? w * 8 : 2304 + w * 10) : w * 18 + (second ? 9 : 0);
                        const rsrc_t rp = mkrsrc(((g2.kh && second) ? g2.wo_ : g2.w2_) + (g2.cb >> 2) * 512, (DFF * D_ - 512) * 2);
                        for (int i = 0; i < n; ++i) bl_lds(rp, lds + JUNK, lane * 16, (k0 + i) * (D_ * 2)); } }
                if (attempt == 0 && st == 2 && hpub >= 0) publish(flags, hpub, T);
                if (attempt == 0 && st == AT_NST - 2) g2_stage(lds, g2, 0, 0);
            } else if (wid < 4) { WAIT_BAR(0); stage(st + 2, (slot == 0) ? 2 : slot - 1); }
            else LGKM_BAR();
            SB0();
            ATILE(kB, vA, kA, vB, nbase, base + 6144);
            slot = nslot;
        }
        o[1] = MFMA32(cat4(vB[0], vB[1]), PF(pb0), o[1]); o[1] = MFMA32(cat4(vB[2], vB[3]), PF(pb1), o[1]);
        ls[1] = MFMA16(sel, PF(pb0), ls[1]); ls[1] = MFMA16(sel, PF(pb1), ls[1]);
#undef ATILE
#undef ASTEP
#undef EXP2
#undef PF
        LGKM_BAR();
        STAMP(2, 4);
        if (attempt == 1) break;
        bool bad = false;
#pragma unroll
        for (int a = 0; a < 2; ++a) { bad |= !(ls[a][0] < 1.0e18f) || !(ls[a][1] < 1.0e18f);
#pragma unroll
            for (int r = 0; r < 16; ++r) bad |= !(fabsf(o[a][r]) < 1.0e30f); }
        LAS unsigned* fl = (LAS unsigned*)(lds + AT_FL);
        if (lane == 0) fl[wid] = __any(bad) ? 1u : 0u;
        LGKM_BAR();
        const u32x4 f0 = *(const LAS u32x4*)fl, f1 = *(const LAS u32x4*)(fl + 4);
        if ((f0[0] | f0[1] | f0[2] | f0[3] | f1[0] | f1[1] | f1[2] | f1[3]) == 0u) break;
        float mx[2] = {-INFINITY, -INFINITY};
        for (int st = 0; st < AT_NST; ++st) {
            if (wid < 4) stage(st, 0);
            WAIT_BAR(0);
#pragma unroll
            for (int h2 = 0; h2 < 2; ++h2)
                for (int kt = 0; kt < AT_KT / 32; ++kt) {
                    const lds_ptr base = lds + h2 * (2 * AT_KT * 64) + kt * 2048;
                    const bf16x8 k0 = lds_b128(base + kr[0]), k1 = lds_b128(base + kr[1]);
#pragma unroll
                    for (int a = 0; a < 2; ++a) { f32x16 s = {}; s = MFMA32(k0, qf[a][0], s); s = MFMA32(k1, qf[a][1], s);
#pragma unroll
                        for (int r = 0; r < 16; ++r) mx[a] = fmaxf(mx[a], s[r]); }
                }
            LGKM_BAR();
        }
#pragma unroll
        for (int a = 0; a < 2; ++a) { auto sw = __builtin_amdgcn_permlane32_swap(__float_as_uint(mx[a]), __float_as_uint(mx[a]), false, false);
            mref[a] = fmaxf(__uint_as_float(sw[0]), __uint_as_float(sw[1])); }
    }
    g2_stage(lds, g2, 1, 1);
    LAS float* xo = (LAS float*)lds + wq * (2 * 17 * 64);
    if (kh == 1) {
#pragma unroll
        for (int a = 0; a < 2; ++a) {
#pragma unroll
            for (int r = 0; r < 16; ++r) xo[(a * 17 + r) * 64 + lane] = o[a][r];
            if (lane < 16) { xo[(a * 17 + 16) * 64 + lane] = ls[a][0]; xo[(a * 17 + 16) * 64 + 16 + lane] = ls[a][1]; } }
    } else {
#pragma unroll
        for (int a = 0; a < 2; ++a) if (lane < 16) { xo[(a * 17 + 16) * 64 + 32 + lane] = ls[a][0]; xo[(a * 17 + 16) * 64 + 48 + lane] = ls[a][1]; }
    }
    LGKM_BAR();
    if (kh == 0) {
        const rsrc_t rsa = mkrsrc(ao, S_ * LDAO * 2);
#pragma unroll
        for (int a = 0; a < 2; ++a) {
            const float l = xo[(a * 17 + 16) * 64 + r32] + xo[(a * 17 + 16) * 64 + 32 + r32];
            const float rl = 1.0f / l;
            unsigned pk[8];
#pragma unroll
            for (int r = 0; r < 8; ++r) pk[r] = cvtpk((o[a][2 * r] + xo[(a * 17 + 2 * r) * 64 + lane]) * rl, (o[a][2 * r + 1] + xo[(a * 17 + 2 * r + 1) * 64 + lane]) * rl);
            const int dst = (qb * 256 + wq * 64 + a * 32 + r32) * LDAO + DFF + head * DH;
#pragma unroll
            for (int k = 0; k < 4; k += 2) {
                auto r0 = __builtin_amdgcn_permlane32_swap(pk[2 * k], pk[2 * k + 2], false, false); auto r1 = __builtin_amdgcn_permlane32_swap(pk[2 * k + 1], pk[2 * k + 3], false, false);
                st_wt((u32x4){r0[0], r1[0], r0[1], r1[1]}, rsa, (unsigned)(dst + 8 * k + 8 * hi) * 2u);
            }
        }
    }
    LGKM_BAR();
    STAMP(2, 5);
}

__device__ __forceinline__ void gemm2_body(lds_ptr lds, const G2& g2, const bf16_t* __restrict__ x, bf16_t* __restrict__ out, float* part, unsigned* flags, const unsigned T, const int rb, const int cb, const int kh, const int opub) {
    constexpr int NKT = 36;
    const int tid = threadIdx.x, lane = tid & 63, wid = __builtin_amdgcn_readfirstlane(tid >> 6), kg = wid >> 2, wm = (wid >> 1) & 1, wn = wid & 1;
    const int fr = lane & 15, g = lane >> 4, q = fr >> 2, p = lane & 3;
    const int m0 = rb * 128, n0 = cb * 128;
    const int ar = (64 * wm + fr) * 128 + (((4 * kg + g) ^ (fr & 7)) * 16);
    int br[4];
    { const int swz = 2 * (q | ((g & 1) << 2));
#pragma unroll
      for (int j = 0; j < 4; ++j) br[j] = 16384 + (32 * kg + 8 * g + q) * 256 + (((2 * (4 * wn + j) + (p >> 1)) ^ swz) * 16) + (p & 1) * 8; }
    f32x4 acc[4][4];
#pragma unroll
    for (int i = 0; i < 4; ++i)
#pragma unroll
        for (int j = 0; j < 4; ++j) acc[i][j] = (f32x4){0.f, 0.f, 0.f, 0.f};
    bf16x8 A0[4], A1[4]; s16x4 B0[4][2], B1[4][2];
    const int wid4 = wid; const rsrc_t rA2 = g2.rA(), rW2 = g2.rW2(), rWo = g2.rWo();
    auto st1 = [&](int t, int sl, int i) {
        const int gt = kh * 36 + t; const lds_ptr d = lds + g2_slot(sl);
        if (i < 2) bl_lds_dev(rA2, d + (16 * wid4 + 8 * i) * 128, g2.aoff, t * 128 + i * (8 * LDAO * 2));
        else { const bool w = gt < 56; bl_lds(w ? rW2 : rWo, d + 16384 + (8 * wid4 + 4 * (i - 2)) * 256, g2.boff[i - 2], (w ? gt : gt - 56) * (64 * D_ * 2)); } };
#define RDONE(AN, BN_, so, r) do { if ((r) < 4) AN[(r) & 3] = lds_b128(lds + (so) + ar + ((r) & 3) * 2048); \
        else if ((r) < 12) BN_[(((r) - 4) >> 1) & 3][(r) & 1] = lds_tr(lds + (so) + br[(((r) - 4) >> 1) & 3] + ((r) & 1) * 1024); } while (0)
#define STEP(AC, BC, AN, BN_, so, tl, sl) do { \
        unroll<0, 16>([&](auto ic) { constexpr int idx = decltype(ic)::value; constexpr int j = idx >> 2, i = idx & 3; \
            acc[i][j] = MFMA16(cat4(BC[j][0], BC[j][1]), AC[i], acc[i][j]); SB0(); \
            RDONE(AN, BN_, so, idx); \
            if ((idx & 3) == 2) st1(tl, sl, idx >> 2); \
            SB0(); }); } while (0)
    STAMP(3, 0);
    g2_stage(lds, g2, 2, 2); g2_stage(lds, g2, 3, 3);
    WAIT_BAR(8);
    publish(flags, opub, T);
    STAMP(3, 1);
    unroll<0, 12>([&](auto ic) { constexpr int idx = decltype(ic)::value; RDONE(A0, B0, g2_slot(0), idx); });
    WAIT_BAR(8);
    for (int t = 0; t < NKT; t += 2) {
        if (kh == 1 && t == 12 && wid == 0) poll_tags(flags, F_O + (rb >> 1) * 32 + (lane & 31), T, 32);
        SB0();
        STEP(A0, B0, A1, B1, g2_slot(t + 1), min(t + 4, NKT - 1), t);
        WAIT_BAR(8); SB0();
        STEP(A1, B1, A0, B0, g2_slot(t + 2), min(t + 5, NKT - 1), t + 1);
        WAIT_BAR(8);
    }
#undef RDONE
#undef STEP
    WAIT_BAR(0);
    STAMP(3, 2);
    LAS f32x4* xr = (LAS f32x4*)lds + (wid & 3) * 1024;
#pragma unroll
    for (int i = 0; i < 2; ++i)
#pragma unroll
        for (int j = 0; j < 4; ++j) xr[(kg * 8 + i * 4 + j) * 64 + lane] = kg ? acc[i][j] : acc[2 + i][j];
    LGKM_BAR();
    f32x4 r[2][4];
#pragma unroll
    for (int i = 0; i < 2; ++i)
#pragma unroll
        for (int j = 0; j < 4; ++j) r[i][j] = (kg ? acc[2 + i][j] : acc[i][j]) + xr[((kg ^ 1) * 8 + i * 4 + j) * 64 + lane];
    const int tile = rb * 8 + cb;
    { const rsrc_t rs = mkrsrc(part + (size_t)(tile * 2 + kh) * 8192, 32768);
#pragma unroll
      for (int j = 0; j < 4; ++j) st_wt(__builtin_bit_cast(u32x4, kh ? r[0][j] : r[1][j]), rs, (wid * 4 + j) * 1024 + lane * 16); }
    WAIT_BAR(0);
    publish(flags, F_P + (tile * 2 + kh) * 32, T);
    const int mrow = m0 + 64 * wm + 32 * kg + 16 * kh + fr;
    u32x2 xv[4];
#pragma unroll
    for (int j = 0; j < 4; ++j) xv[j] = *(const u32x2*)(x + (size_t)mrow * D_ + n0 + 64 * wn + 16 * j + 4 * g);
    if (wid == 0) poll_tags(flags, F_P + (tile * 2 + (kh ^ 1)) * 32, T, 1);
    __syncthreads();
    STAMP(3, 3);
    { const rsrc_t rs = mkrsrc(part + (size_t)(tile * 2 + (kh ^ 1)) * 8192, 32768);
      f32x4 o[4];
#pragma unroll
      for (int j = 0; j < 4; ++j) o[j] = __builtin_bit_cast(f32x4, __builtin_amdgcn_raw_buffer_load_b128(rs, (wid * 4 + j) * 1024 + lane * 16, 0, 16));
      unsigned pk[4][2];
#pragma unroll
      for (int j = 0; j < 4; ++j) { f32x4 v = (kh ? r[1][j] : r[0][j]) + o[j];
          v[0] += __uint_as_float(xv[j][0] << 16); v[1] += __uint_as_float(xv[j][0] & 0xffff0000u); v[2] += __uint_as_float(xv[j][1] << 16); v[3] += __uint_as_float(xv[j][1] & 0xffff0000u);
          pk[j][0] = cvtpk(v[0], v[1]); pk[j][1] = cvtpk(v[2], v[3]); }
#pragma unroll
      for (int j = 0; j < 4; j += 2) {
          auto r0 = __builtin_amdgcn_permlane16_swap(pk[j][0], pk[j + 1][0], false, false); auto r1 = __builtin_amdgcn_permlane16_swap(pk[j][1], pk[j + 1][1], false, false);
          *(u32x4*)(out + (size_t)mrow * D_ + n0 + 64 * wn + 16 * (j + (g & 1)) + 8 * (g >> 1)) = (u32x4){r0[0], r1[0], r0[1], r1[1]}; } }
    STAMP(3, 4);
}

__global__ __launch_bounds__(512, 2) void fused_kernel(const bf16_t* __restrict__ wqkv, const bf16_t* __restrict__ w1, const bf16_t* __restrict__ w2, const bf16_t* __restrict__ wo,
                                                       const bf16_t* __restrict__ x, bf16_t* qkv, bf16_t* ao, bf16_t* __restrict__ out, float* part, unsigned* flags) {
    __shared__ __attribute__((aligned(16))) char smem[LDS_BYTES];
    const lds_ptr lds = (lds_ptr)smem;
    const int xg = blockIdx.x & 7, r = blockIdx.x >> 3;
    const int rb1 = (xg & 1) * 4 + (r & 3), c1 = (xg >> 1) * 4 + ((r >> 2) & 3), ffn = r >> 4;
    unsigned T;
    if (ffn) T = gemm1_body<7>(lds, x, w1, DFF, rb1 * 256, c1 * 224, qkv, ao, flags, 16 + c1, wqkv, w1);
    else { T = gemm1_body<6>(lds, x, wqkv, 3 * D_, rb1 * 256, c1 * 192, qkv, ao, flags, c1, wqkv, w1); publish(flags, F_Q + c1 * 32 + rb1, T); }
    const int kh = xg & 1, rb3 = ((xg >> 1) & 1) * 8 + (r & 7), cb3 = (xg >> 2) * 4 + (r >> 3);
    const G2 g2 = g2_setup(ao, w2, wo, rb3, cb3, kh);
    const int head = xg * 4 + (r >> 3), qb = r & 7;
    attn_body(lds, qkv, ao, flags, T, head, qb, ffn ? F_H + rb1 * 32 + c1 : -1, F_H + (rb3 >> 1) * 32, g2);
    gemm2_body(lds, g2, x, out, part, flags, T, rb3, cb3, kh, F_O + qb * 32 + head);
}

extern "C" void kernel_launch(void* const* d_in, const int* in_sizes, int n_in, void* d_out, int out_size, void* d_ws, size_t ws_size, hipStream_t stream) {
    const bf16_t* x = (const bf16_t*)d_in[0]; const bf16_t* wqkv = (const bf16_t*)d_in[1]; const bf16_t* wo = (const bf16_t*)d_in[2];
    const bf16_t* w1 = (const bf16_t*)d_in[4]; const bf16_t* w2 = (const bf16_t*)d_in[5];
    char* ws = (char*)d_ws;
    unsigned* flags = (unsigned*)(ws + WS_FLAGS); bf16_t* qkv = (bf16_t*)(ws + WS_QKV); bf16_t* ao = (bf16_t*)(ws + WS_AO); float* part = (float*)(ws + WS_PART);
    fused_kernel<<<256, 512, 0, stream>>>(wqkv, w1, w2, wo, x, qkv, ao, (bf16_t*)d_out, part, flags);
}
```

```cpp
#include <hip/hip_runtime.h>
#include <hip/hip_bf16.h>
#include <stdint.h>

typedef unsigned short bf16_t;
typedef short bf16x8 __attribute__((ext_vector_type(8)));
typedef short s16x4 __attribute__((ext_vector_type(4)));
typedef float f32x2 __attribute__((ext_vector_type(2)));
typedef float f32x4 __attribute__((ext_vector_type(4)));
typedef float f32x16 __attribute__((ext_vector_type(16)));
typedef unsigned u32x4 __attribute__((ext_vector_type(4)));
typedef unsigned u32x2 __attribute__((ext_vector_type(2)));
typedef __bf16 bf16x2n __attribute__((ext_vector_type(2)));
typedef __bf16 bf16x4n __attribute__((ext_vector_type(4)));
#define LAS __attribute__((address_space(3)))
typedef LAS char* lds_ptr;

constexpr int S_ = 2048, D_ = 1024, H_ = 32, DH = 32, DFF = 3584;
constexpr int LDAO = DFF + D_;
constexpr size_t WS_FLAGS = 0;
constexpr size_t WS_QKV = 1u << 20;
constexpr size_t WS_AO = WS_QKV + (size_t)3 * S_ * D_ * 2;
constexpr size_t WS_PART = WS_AO + (size_t)S_ * LDAO * 2;
constexpr int LDS_BYTES = 163840;
constexpr int F_E = 0, F_ST = 256, F_Q = 1024, F_H = 2048, F_O = 3072, F_P = 4096;
constexpr int F_LT = 512, F_CT = 768;
constexpr int F_LN = 16384, F_CS = 32768;
constexpr int NCOL = 3 * D_ + DFF;
#define SPLITQ 40
constexpr int TBL = 151552;

#define EXP_NOADD 0
#define EXP_NOROT 0
#define EXP_NOHST 0
#define STAMP(k, i) do {} while (0)

__device__ __forceinline__ unsigned cvtpk(float lo, float hi) { return __builtin_bit_cast(unsigned, __builtin_convertvector((f32x2){lo, hi}, bf16x2n)); }
__device__ __forceinline__ float rbf(float x) { return __uint_as_float(cvtpk(x, x) << 16); }
typedef __amdgpu_buffer_rsrc_t rsrc_t;
__device__ __forceinline__ rsrc_t mkrsrc(const void* p, unsigned bytes) { return __builtin_amdgcn_make_buffer_rsrc((void*)p, 0, bytes, 0x00020000); }
__device__ __forceinline__ void bl_lds(rsrc_t r, lds_ptr l, unsigned voff, unsigned soff) {
    asm volatile("s_mov_b32 m0, %0\n\ts_nop 0\n\tbuffer_load_dwordx4 %1, %2, %3 offen lds" :: "s"(__builtin_amdgcn_readfirstlane((unsigned)(uintptr_t)l)), "v"(voff), "s"(r), "s"(__builtin_amdgcn_readfirstlane(soff)) : "memory"); }
__device__ __forceinline__ void bl_lds_dev(rsrc_t r, lds_ptr l, unsigned voff, unsigned soff) {
    asm volatile("s_mov_b32 m0, %0\n\ts_nop 0\n\tbuffer_load_dwordx4 %1, %2, %3 offen sc1 lds" :: "s"(__builtin_amdgcn_readfirstlane((unsigned)(uintptr_t)l)), "v"(voff), "s"(r), "s"(__builtin_amdgcn_readfirstlane(soff)) : "memory"); }
__device__ __forceinline__ void st_wt(u32x4 v, rsrc_t r, unsigned off) { __builtin_amdgcn_raw_buffer_store_b128(v, r, off, 0, 16); }
__device__ __forceinline__ bf16x8 lds_b128(lds_ptr p) { return *(const LAS bf16x8*)p; }
__device__ __forceinline__ s16x4 lds_tr(lds_ptr p) { return __builtin_amdgcn_ds_read_tr16_b64_v4i16((LAS s16x4*)p); }
__device__ __forceinline__ bf16x8 cat4(s16x4 a, s16x4 b) { return (bf16x8){a[0], a[1], a[2], a[3], b[0], b[1], b[2], b[3]}; }
#define MFMA16(a, b, c) __builtin_amdgcn_mfma_f32_16x16x32_bf16(a, b, c, 0, 0, 0)
#define MFMA32(a, b, c) __builtin_amdgcn_mfma_f32_32x32x16_bf16(a, b, c, 0, 0, 0)
#define WAIT_BAR(N) asm volatile("s_waitcnt vmcnt(%0) lgkmcnt(0)\n\ts_barrier" :: "n"(N) : "memory")
#define LGKM_BAR() asm volatile("s_waitcnt lgkmcnt(0)\n\ts_barrier" ::: "memory")
#define SB0() __builtin_amdgcn_sched_barrier(0)
#define RLX_AGENT __ATOMIC_RELAXED, __HIP_MEMORY_SCOPE_AGENT
template <int I> struct IC { static constexpr int value = I; };
template <int B, int E, class F> __device__ __forceinline__ void unroll(F&& f) { if constexpr (B < E) { f(IC<B>{}); unroll<B + 1, E>(f); } }

__device__ __forceinline__ float wave_sum(float v) {
    v += __builtin_bit_cast(float, __builtin_amdgcn_update_dpp(0, __builtin_bit_cast(int, v), 0xB1, 0xf, 0xf, true));
    v += __builtin_bit_cast(float, __builtin_amdgcn_update_dpp(0, __builtin_bit_cast(int, v), 0x4E, 0xf, 0xf, true));
    v += __builtin_bit_cast(float, __builtin_amdgcn_update_dpp(0, __builtin_bit_cast(int, v), 0x141, 0xf, 0xf, true));
    v += __builtin_bit_cast(float, __builtin_amdgcn_update_dpp(0, __builtin_bit_cast(int, v), 0x140, 0xf, 0xf, true));
    { auto r = __builtin_amdgcn_permlane16_swap(__float_as_uint(v), __float_as_uint(v), false, false); v = __uint_as_float(r[0]) + __uint_as_float(r[1]); }
    { auto r = __builtin_amdgcn_permlane32_swap(__float_as_uint(v), __float_as_uint(v), false, false); v = __uint_as_float(r[0]) + __uint_as_float(r[1]); }
    return v;
}
__device__ __forceinline__ void poll_tags(unsigned* flags, int word, unsigned T, int n) {
    const bool act = (int)(threadIdx.x & 63) < n; unsigned spins = 0;
    for (;;) { unsigned v = T; if (act) v = __hip_atomic_load(flags + word, RLX_AGENT);
        if (__all(v == T)) break; __builtin_amdgcn_s_sleep(2); if (++spins > (1u << 22)) break; }
    asm volatile("" ::: "memory");
}
__device__ __forceinline__ void publish(unsigned* flags, int word, unsigned T) {
    if (threadIdx.x == 0) __hip_atomic_store(flags + word, T, RLX_AGENT);
}
typedef unsigned long long u64;
__device__ __forceinline__ void put_granule(unsigned* flags, int word, float v, unsigned T) { __hip_atomic_store((u64*)(flags + word), ((u64)T << 32) | __float_as_uint(v), RLX_AGENT); }
__device__ __forceinline__ u64 get_granule(unsigned* flags, int word) { return __hip_atomic_load((u64*)(flags + word), RLX_AGENT); }

constexpr int JUNK = 162816;
constexpr int HSTG = 65536;
template <int NT>
__device__ __forceinline__ unsigned gemm1_body(lds_ptr lds, const bf16_t* xn, const bf16_t* W, const int ldw, const int m0, const int n0, bf16_t* qkv, bf16_t* ao, unsigned* flags, const int j32, const bf16_t* wq_, const bf16_t* w1_) {
    constexpr int BN = 32 * NT, RB = BN * 2, CPR = BN / 8, BSLOT = 64 * RB, NKT = D_ / 64, GB = (NT == 7) ? 4 : 3;
    constexpr int LB = 65536, NMF = 4 * NT, NRD = 4 + 2 * NT, NBB = (NT == 7) ? 2 : 4;
    const int tid = threadIdx.x, lane = tid & 63, wid = __builtin_amdgcn_readfirstlane(tid >> 6), wm = wid & 3, wn = wid >> 2;
    const int fr = lane & 15, g = lane >> 4, q = fr >> 2, p = lane & 3;
    const rsrc_t rA = mkrsrc(xn + (size_t)m0 * D_, 256 * D_ * 2), rB = mkrsrc(W + n0, (unsigned)(D_ * ldw - n0) * 2u);
    const unsigned aoff = (unsigned)((32 * wid + (lane >> 3)) * D_ + (((lane & 7) ^ ((lane >> 3) & 7)) * 8)) * 2u;
    unsigned boff[GB];
#pragma unroll
    for (int i = 0; i < GB; ++i) { const int idx = 64 * i + lane, rl = idx / CPR, cp = idx % CPR, k = (8 * wid + rl) & 63;
        const int swz = (NT == 7) ? 2 * ((k >> 3) & 1) : 2 * (((k >> 1) & 1) | (((k >> 3) & 1) << 1));
        boff[i] = (unsigned)(k * ldw + (cp ^ swz) * 8) * 2u; }
    const int krot = (m0 >> 10) * 8, tcs = ((m0 >> 8) & 3) * 2;
    auto stageA1 = [&](int kt_, int slot, int i) { const int kt = (kt_ + krot) & (NKT - 1); bl_lds(rA, lds + slot * 32768 + (32 * wid + 8 * i) * 128, aoff, kt * 128 + i * (8 * D_ * 2)); };
    auto stageB1 = [&](int kt_, int slot, int i) { const int kt = (kt_ + krot) & (NKT - 1); const unsigned so = (unsigned)(kt * 64 * ldw) * 2u;
        if (NT == 7 && i == 3) { if (lane < 32) bl_lds(rB, lds + LB + slot * BSLOT + (8 * wid * CPR + 192) * 16, boff[GB - 1], so); }
        else bl_lds(rB, lds + LB + slot * BSLOT + (8 * wid * CPR + 64 * i) * 16, boff[i < GB ? i : 0], so); };
    int ar[2];
#pragma unroll
    for (int ks = 0; ks < 2; ++ks) ar[ks] = (64 * wm + fr) * 128 + (((4 * ks + g) ^ (fr & 7)) * 16);
    int br[NBB];
    { const int swz = (NT == 7) ? 2 * (g & 1) : 2 * ((q >> 1) | ((g & 1) << 1));
#pragma unroll
      for (int j = 0; j < NBB; ++j) br[j] = LB + (8 * g + q) * RB + (((2 * (NT * wn + j) + (p >> 1)) ^ swz) * 16) + (p & 1) * 8; }
    f32x4 acc[4][NT];
#pragma unroll
    for (int i = 0; i < 4; ++i)
#pragma unroll
        for (int j = 0; j < NT; ++j) acc[i][j] = (f32x4){0.f, 0.f, 0.f, 0.f};
    bf16x8 A0[4], A1[4]; s16x4 B0[NT][2], B1[NT][2];
#define RDONE(AN, BN_, sa, sb, ks, r) do { if ((r) < 4) AN[(r) < 4 ? (r) : 0] = lds_b128(lds + (sa) + ar[ks] + ((r) < 4 ? (r) : 0) * 2048); \
        else if ((r) < NRD) { constexpr int jj = (((r) - 4) >> 1) < NT && (r) >= 4 ? (((r) - 4) >> 1) : 0, tt = ((r) - 4) & 1; BN_[jj][tt] = lds_tr(lds + (sb) + br[jj % NBB] + 32 * (jj - jj % NBB) + (32 * (ks) + 4 * tt) * RB); } } while (0)
#define SUBSTEP(AC, BC, AN, BN_, sa, sb, ks, STG, go, tl, sl) do { \
        unroll<0, NMF>([&](auto ic) { constexpr int idx = decltype(ic)::value; constexpr int j = idx >> 2, i = idx & 3; \
            acc[i][j] = MFMA16(cat4(BC[j][0], BC[j][1]), AC[i], acc[i][j]); SB0(); \
            RDONE(AN, BN_, sa, sb, ks, idx); \
            if ((idx % NT) == 1 && (go)) { if (STG == 1) stageA1(tl, sl, idx / NT); if (STG == 2 && idx / NT < GB) stageB1(tl, sl, idx / NT); } \
            SB0(); }); } while (0)
    STAMP(1, 0);
#pragma unroll
    for (int t = 0; t < 2; ++t) {
#pragma unroll
        for (int i = 0; i < GB; ++i) stageB1(t, t, i);
#pragma unroll
        for (int i = 0; i < 4; ++i) stageA1(t, t, i); }
    unsigned T; float lnv;
    const int ncg = (NT == 7 ? 3 * D_ : 0) + n0;
    {   const int row = m0 + 8 * j32 + wid;
        const u32x4* src = (const u32x4*)(xn + (size_t)row * D_);
        u32x4 v0, v1; unsigned ev;
        asm volatile("global_load_dwordx4 %0, %3, off\n\tglobal_load_dwordx4 %1, %3, off offset:1024\n\tglobal_load_dword %2, %4, off sc1"
                     : "=&v"(v0), "=&v"(v1), "=&v"(ev) : "v"(src + lane), "v"(flags + F_E) : "memory");
        asm volatile("s_waitcnt vmcnt(0)" : "+v"(v0), "+v"(v1), "+v"(ev) :: "memory");
        SB0();
        float f[16];
#pragma unroll
        for (int i = 0; i < 4; ++i) { f[2 * i] = __uint_as_float(v0[i] << 16); f[2 * i + 1] = __uint_as_float(v0[i] & 0xffff0000u); f[8 + 2 * i] = __uint_as_float(v1[i] << 16); f[8 + 2 * i + 1] = __uint_as_float(v1[i] & 0xffff0000u); }
        float sm = 0.f;
#pragma unroll
        for (int i = 0; i < 16; ++i) sm += f[i];
        const float mean = wave_sum(sm) * (1.0f / D_);
        float qq = 0.f;
#pragma unroll
        for (int i = 0; i < 16; ++i) { const float d = f[i] - mean; qq += d * d; }
        qq = wave_sum(qq);
        T = (unsigned)__builtin_amdgcn_readfirstlane(ev) + 1u;
        const float sd = rbf(__fsqrt_rn(rbf(rbf(qq * (1.0f / D_)) + 1e-5f)));
        lnv = (lane & 1) ? 1.0f / sd : rbf(mean);
    }
    WAIT_BAR(0);
    auto colsum = [&](s16x4 (&BC)[NT][2]) {
        const bf16x8 ones = {0x3f80, 0x3f80, 0x3f80, 0x3f80, 0x3f80, 0x3f80, 0x3f80, 0x3f80}; const f32x4 z = {0.f, 0.f, 0.f, 0.f};
        float c[NT];
#pragma unroll
        for (int j = 0; j < NT; ++j) c[j] = MFMA16(ones, cat4(BC[j][0], BC[j][1]), z)[0];
        if (lane < 16) {
#pragma unroll
            for (int j = 0; j < NT; ++j) ((LAS float*)(lds + TBL + 2048))[wm * 256 + 16 * (NT * wn + j) + lane] = c[j]; } };
    STAMP(1, 1);
    unroll<0, NRD>([&](auto ic) { constexpr int idx = decltype(ic)::value; RDONE(A0, B0, 0, 0, 0, idx); });
    int sa = 0, sb = 0, sb3 = 0;
    const rsrc_t rF = mkrsrc(flags, 1u << 20);
    unsigned long long itv = 0;
    const unsigned evmask = 1u | (1u << (tcs + (wm >> 1))) | (1u << (tcs + 3)) | (1u << (NKT - 2)) | (wid == 0 ? (4u | (1u << (tcs + 5)) | (1u << (NKT - 3))) : 0u);
    for (int t = 0; t < NKT; ++t) {
        const int tn = min(t + 2, NKT - 1); constexpr bool go = true;
        SB0();
        { const int slb = (sb3 == 0) ? 2 : sb3 - 1;
          SUBSTEP(A0, B0, A1, B1, sa, sb, 1, 2, go, tn, slb); }
        if (__builtin_expect((evmask >> t) & 1u, 0)) {
            if (t == 0) { if (lane < 2) __hip_atomic_store(flags + F_LN + (m0 + 8 * j32 + wid) * 2 + lane, __float_as_uint(lnv), RLX_AGENT);
                if (wid == 0) { publish(flags, F_ST + blockIdx.x, T); WAIT_BAR(GB + 2); } else WAIT_BAR(GB + 1); }
            else if (wid == 0 && (t == 2 || t == tcs + 5)) { publish(flags, (t == 2 ? F_LT : F_CT) + (m0 >> 8) * 32 + j32, T); WAIT_BAR(GB + 1); }
            else if (wid * 64 < BN && t == tcs + 3) { if (tid < BN) { const LAS float* pc = (const LAS float*)(lds + TBL + 2048) + tid;
                    __hip_atomic_store(flags + F_CS + (m0 >> 8) * NCOL + ncg + tid, __float_as_uint((pc[0] + pc[256]) + (pc[512] + pc[768])), RLX_AGENT); }
                WAIT_BAR(GB + 1); }
            else WAIT_BAR(GB);
            if (t == tcs + (wm >> 1)) { if (wm & 1) colsum(B1); else colsum(B0); }
            if (t == NKT - 3 && wid == 0) poll_tags(flags, lane < 32 ? F_LT + (m0 >> 8) * 32 + lane : F_CT + (lane & 7) * 32 + j32, T, 40);
            if (t == NKT - 2) {
                if (lane < BN / 4) bl_lds_dev(rF, lds + TBL + 2048 + wid * 1024, lane * 16, (F_CS + wid * NCOL + ncg) * 4);
                if (wid < 2) bl_lds_dev(rF, lds + TBL + wid * 1024, lane * 16, (F_LN + m0 * 2) * 4 + wid * 1024); }
        } else WAIT_BAR(GB);
        SB0();
        sa ^= 32768; sb3 = (sb3 == 2) ? 0 : sb3 + 1; sb = sb3 * BSLOT;
        SUBSTEP(A1, B1, A0, B0, sa, sb, 0, 1, go, tn, t & 1);
    }
#undef RDONE
#undef SUBSTEP
    STAMP(1, 2);
    if (tid < BN) { float cs = 0.f;
#pragma unroll
        for (int i = 0; i < 8; ++i) cs += ((const LAS float*)(lds + TBL + 2048))[i * 256 + tid];
        ((LAS float*)(lds + TBL + 10240))[tid] = cs; }
    WAIT_BAR(0);
#pragma unroll
    for (int i = 0; i < 4; ++i) { const f32x2 st = ((const LAS f32x2*)(lds + TBL))[64 * wm + 16 * i + fr];
#pragma unroll
        for (int j = 0; j < NT; ++j) { const f32x4 cs = *(const LAS f32x4*)(lds + TBL + 10240 + (16 * (NT * wn + j) + 4 * g) * 4); acc[i][j] = (acc[i][j] - cs * st[0]) * st[1]; } }
    const int mrow = m0 + 64 * wm + fr;
    if constexpr (NT == 6) {
        const rsrc_t rsq = mkrsrc(qkv, 3 * S_ * D_ * 2);
#pragma unroll
        for (int b = 0; b < 3; ++b) {
            const int col = n0 + 96 * wn + 32 * b, which = col >> 10, head = (col & 1023) >> 5;
            const float sc = (which == 0) ? 0.17677669529663687f * 1.4426950408889634f : 1.0f;
            const int dst = ((which * H_ + head) * S_) * DH;
#pragma unroll
            for (int i = 0; i < 4; ++i) {
                const f32x4 u = acc[i][2 * b] * sc, v = acc[i][2 * b + 1] * sc;
                unsigned x0 = cvtpk(u[0], u[1]), x1 = cvtpk(u[2], u[3]), y0 = cvtpk(v[0], v[1]), y1 = cvtpk(v[2], v[3]);
                auto r0 = __builtin_amdgcn_permlane16_swap(x0, y0, false, false); auto r1 = __builtin_amdgcn_permlane16_swap(x1, y1, false, false);
                const int tok = mrow + 16 * i; int ch = 2 * (g & 1) + (g >> 1); if (which == 1) ch ^= (tok >> 2) & 3;
                st_wt((u32x4){r0[0], r1[0], r0[1], r1[1]}, rsq, (unsigned)(dst + tok * DH + ch * 8) * 2u);
            }
        }
        WAIT_BAR(0);
    } else {
        const rsrc_t rsa = mkrsrc(ao, S_ * LDAO * 2);
        u32x4 sv[14];
        auto pk = [&](const f32x4& a, const f32x4& b) { const f32x4 z = {0.f, 0.f, 0.f, 0.f}; const f32x4 u = __builtin_elementwise_max(a, z), v = __builtin_elementwise_max(b, z);
            unsigned x0 = cvtpk(u[0], u[1]), x1 = cvtpk(u[2], u[3]), y0 = cvtpk(v[0], v[1]), y1 = cvtpk(v[2], v[3]);
            auto r0 = __builtin_amdgcn_permlane16_swap(x0, y0, false, false); auto r1 = __builtin_amdgcn_permlane16_swap(x1, y1, false, false); return (u32x4){r0[0], r1[0], r0[1], r1[1]}; };
        auto off = [&](int s, int half) -> unsigned {
            const int c0 = n0 + 112 * half;
            if (s < 12) return (unsigned)((mrow + 16 * (s & 3)) * LDAO + c0 + 16 * half + 32 * (s >> 2) + 16 * (g & 1) + 8 * (g >> 1)) * 2u;
            return (unsigned)((mrow + 16 * (2 * (s - 12) + (g & 1))) * LDAO + c0 + (half ? 0 : 96) + 8 * (g >> 1)) * 2u; };
        if (wn == 0) {
#pragma unroll
            for (int b = 0; b < 3; ++b)
#pragma unroll
                for (int i = 0; i < 4; ++i) sv[4 * b + i] = pk(acc[i][2 * b], acc[i][2 * b + 1]);
#pragma unroll
            for (int i = 0; i < 2; ++i) sv[12 + i] = pk(acc[2 * i][6], acc[2 * i + 1][6]);
#pragma unroll
            for (int s = 0; s < 14; ++s) *(LAS u32x4*)(lds + HSTG + (wm * 14 + s) * 1024 + lane * 16) = sv[s];
            LGKM_BAR();
        } else {
#pragma unroll
            for (int b = 0; b < 3; ++b)
#pragma unroll
                for (int i = 0; i < 4; ++i) sv[4 * b + i] = pk(acc[i][2 * b + 1], acc[i][2 * b + 2]);
#pragma unroll
            for (int i = 0; i < 2; ++i) sv[12 + i] = pk(acc[2 * i][0], acc[2 * i + 1][0]);
#pragma unroll
            for (int s = 0; s < 14; ++s) if (!EXP_NOHST) st_wt(sv[s], rsa, off(s, 1));
            LGKM_BAR();
#pragma unroll
            for (int s = 0; s < 14; ++s) sv[s] = *(const LAS u32x4*)(lds + HSTG + (wm * 14 + s) * 1024 + lane * 16);
#pragma unroll
            for (int s = 0; s < 14; ++s) if (!EXP_NOHST || sv[s][0] == 0x12345678) st_wt(sv[s], rsa, off(s, 0));
        }
    }
    STAMP(1, 3);
    return T;
}

struct G2 { const bf16_t *ao_, *w2_, *wo_; unsigned aoff, boff[2]; int rb, cb, kh, k0, nkt;
    __device__ __forceinline__ rsrc_t rA() const { return mkrsrc(ao_ + (size_t)rb * 128 * LDAO + k0 * 64, (unsigned)(128 * LDAO - k0 * 64) * 2u); }
    __device__ __forceinline__ rsrc_t rW2() const { return mkrsrc(w2_ + cb * 128, (unsigned)(DFF * D_ - cb * 128) * 2u); }
    __device__ __forceinline__ rsrc_t rWo() const { return mkrsrc(wo_ + cb * 128, (unsigned)(D_ * D_ - cb * 128) * 2u); } };
__device__ __forceinline__ G2 g2_setup(const bf16_t* ao, const bf16_t* w2, const bf16_t* wo, int rb, int cb, int kh, int split) {
    const int lane = threadIdx.x & 63, wid = __builtin_amdgcn_readfirstlane(threadIdx.x >> 6);
    G2 s; s.kh = kh; s.k0 = kh ? split : 0; s.nkt = kh ? 72 - split : split; s.rb = rb; s.cb = cb; s.ao_ = ao; s.w2_ = w2; s.wo_ = wo;
    s.aoff = (unsigned)((16 * wid + (lane >> 3)) * LDAO + (((lane & 7) ^ ((lane >> 3) & 7)) * 8)) * 2u;
#pragma unroll
    for (int i = 0; i < 2; ++i) { const int k = 8 * wid + 4 * i + (lane >> 4), cp = lane & 15, swz = 2 * ((k & 3) | (((k >> 3) & 1) << 2)); s.boff[i] = (unsigned)(k * D_ + (cp ^ swz) * 8) * 2u; }
    return s;
}
__device__ __forceinline__ int g2_slot(int t) { return (int)((0x10002030u >> (8 * (t & 3))) & 0xffu) * 2048; }
__device__ __forceinline__ void g2_stage(lds_ptr lds, const G2& s, int t, int sl) {
    const int wid = __builtin_amdgcn_readfirstlane(threadIdx.x >> 6), gt = s.k0 + t; const lds_ptr d = lds + g2_slot(sl);
#pragma unroll
    for (int i = 0; i < 2; ++i) bl_lds_dev(s.rA(), d + (16 * wid + 8 * i) * 128, s.aoff, t * 128 + i * (8 * LDAO * 2));
    if (gt < 56) {
#pragma unroll
        for (int i = 0; i < 2; ++i) bl_lds(s.rW2(), d + 16384 + (8 * wid + 4 * i) * 256, s.boff[i], gt * (64 * D_ * 2));
    } else {
#pragma unroll
        for (int i = 0; i < 2; ++i) bl_lds(s.rWo(), d + 16384 + (8 * wid + 4 * i) * 256, s.boff[i], (gt - 56) * (64 * D_ * 2));
    }
}

constexpr int AT_KT = 128, AT_STAGE = 4 * AT_KT * 64, AT_NST = 1024 / AT_KT, AT_Q = 122880, AT_FL = 139264;
__device__ __forceinline__ void attn_body(lds_ptr lds, const bf16_t* qkv, bf16_t* ao, unsigned* flags, const unsigned T, const int head, const int qb, const int hpub, const int hwait, const G2& g2) {
    const int tid = threadIdx.x, lane = tid & 63, wid = __builtin_amdgcn_readfirstlane(tid >> 6), wq = wid & 3, kh = wid >> 2;
    const int r32 = lane & 31, hi = lane >> 5;
    const rsrc_t rs = mkrsrc(qkv, 3 * S_ * D_ * 2);
    const unsigned ssrc = (unsigned)((((1 + (wid & 1)) * H_ + head) * S_ + (wid >> 1) * 1024) * 64);
    auto stage = [&](int st, int slot) {
#pragma unroll
        for (int i = 0; i < 8; ++i) bl_lds_dev(rs, lds + slot * AT_STAGE + (wid & 3) * 8192 + i * 1024, lane * 16, ssrc + st * (AT_KT * 64) + i * 1024); };
    STAMP(2, 0);
    if (wid < 4) {
        poll_tags(flags, F_Q + (((lane >> 3) & 3) * 1024 + head * 32) / 192 * 32 + (lane & 7), T, 24);
        STAMP(2, 1);
#pragma unroll
        for (int i = 0; i < 4; ++i) bl_lds_dev(rs, lds + AT_Q + wid * 4096 + i * 1024, lane * 16, (unsigned)((head * S_ + qb * 256) * 64 + wid * 4096 + i * 1024));
    }
    const int kr[2] = {r32 * 64 + ((hi ^ ((r32 >> 2) & 3)) * 16), r32 * 64 + (((2 + hi) ^ ((r32 >> 2) & 3)) * 16)};
    const int vr = AT_KT * 64 + (4 * hi + ((lane & 15) >> 2)) * 64 + ((lane >> 4) & 1) * 32 + (lane & 3) * 8;
    bf16x8 sel;
    { const short one = ((lane & 15) == ((lane >> 4) & 1)) ? (short)0x3f80 : (short)0;
#pragma unroll
      for (int j = 0; j < 8; ++j) sel[j] = one; }
    bf16x8 qf[2][2];
    float mref[2];
    f32x16 o[2]; f32x4 ls[2];
    for (int attempt = 0; attempt < 2; ++attempt) {
        if (wid < 4) { stage(0, 0); stage(1, 1); asm volatile("s_waitcnt vmcnt(8)" ::: "memory"); STAMP(2, 2); WAIT_BAR(8); } else LGKM_BAR();
        if (attempt == 0) {
#pragma unroll
            for (int a = 0; a < 2; ++a)
#pragma unroll
                for (int s = 0; s < 2; ++s) qf[a][s] = lds_b128(lds + AT_Q + (wq * 64 + a * 32 + r32) * 64 + (2 * s + hi) * 16);
            const bf16x8 k0 = lds_b128(lds + kr[0]), k1 = lds_b128(lds + kr[1]);
#pragma unroll
            for (int a = 0; a < 2; ++a) { f32x16 s = {}; s = MFMA32(k0, qf[a][0], s); s = MFMA32(k1, qf[a][1], s);
                float m = s[0];
#pragma unroll
                for (int r = 1; r < 16; ++r) m = fmaxf(m, s[r]);
                auto sw = __builtin_amdgcn_permlane32_swap(__float_as_uint(m), __float_as_uint(m), false, false);
                mref[a] = fmaxf(__uint_as_float(sw[0]), __uint_as_float(sw[1])); }
        }
        f32x16 nm[2];
#pragma unroll
        for (int a = 0; a < 2; ++a) {
#pragma unroll
            for (int r = 0; r < 16; ++r) nm[a][r] = -mref[a];
            o[a] = (f32x16){}; ls[a] = (f32x4){}; }
        bf16x8 kA[2], kB[2]; s16x4 vA[4], vB[4]; f32x16 sa_, sb_; u32x4 pa0, pa1, pb0, pb1;
        { const lds_ptr base = lds + kh * (2 * AT_KT * 64);
          kA[0] = lds_b128(base + kr[0]); kA[1] = lds_b128(base + kr[1]);
          sa_ = MFMA32(kA[0], qf[0][0], nm[0]); sa_ = MFMA32(kA[1], qf[0][1], sa_);
          pb0 = (u32x4){0u, 0u, 0u, 0u}; pb1 = pb0;
#pragma unroll
          for (int i = 0; i < 4; ++i) vB[i] = (s16x4){0, 0, 0, 0}; }
#define EXP2(S, i) S[i] = __builtin_amdgcn_exp2f(S[i])
#define PF(x) __builtin_bit_cast(bf16x8, x)
#define ASTEP(SC, PC0, PC1, SN, KF, QN, NMN, ON, LN_, VF, PP0, PP1, R0, R1, R2, R3, R4, R5) do { \
            SN = MFMA32(KF[0], QN[0], NMN); SB0(); EXP2(SC, 0); EXP2(SC, 1); EXP2(SC, 2); R0; SB0(); \
            SN = MFMA32(KF[1], QN[1], SN); SB0(); EXP2(SC, 3); EXP2(SC, 4); EXP2(SC, 5); PC0[0] = cvtpk(SC[0], SC[1]); R1; SB0(); \
            ON = MFMA32(cat4(VF[0], VF[1]), PF(PP0), ON); SB0(); EXP2(SC, 6); EXP2(SC, 7); EXP2(SC, 8); PC0[1] = cvtpk(SC[2], SC[3]); R2; SB0(); \
            ON = MFMA32(cat4(VF[2], VF[3]), PF(PP1), ON); SB0(); EXP2(SC, 9); EXP2(SC, 10); EXP2(SC, 11); PC0[2] = cvtpk(SC[4], SC[5]); R3; SB0(); \
            LN_ = MFMA16(sel, PF(PP0), LN_); SB0(); EXP2(SC, 12); EXP2(SC, 13); PC0[3] = cvtpk(SC[6], SC[7]); PC1[0] = cvtpk(SC[8], SC[9]); R4; SB0(); \
            LN_ = MFMA16(sel, PF(PP1), LN_); SB0(); EXP2(SC, 14); EXP2(SC, 15); PC1[1] = cvtpk(SC[10], SC[11]); PC1[2] = cvtpk(SC[12], SC[13]); R5; PC1[3] = cvtpk(SC[14], SC[15]); asm volatile("" : "+v"(PC0), "+v"(PC1)); SB0(); } while (0)
#define ATILE(KC, VP, KN, VN, kb, vb) do { \
            ASTEP(sa_, pa0, pa1, sb_, KC, qf[1], nm[1], o[1], ls[1], VP, pb0, pb1, KN[0] = lds_b128((kb) + kr[0]), KN[1] = lds_b128((kb) + kr[1]), \
                  VN[0] = lds_tr((vb) + vr), VN[1] = lds_tr((vb) + vr + 512), VN[2] = lds_tr((vb) + vr + 1024), VN[3] = lds_tr((vb) + vr + 1536)); \
            ASTEP(sb_, pb0, pb1, sa_, KN, qf[0], nm[0], o[0], ls[0], VN, pa0, pa1, (void)0, (void)0, (void)0, (void)0, (void)0, (void)0); } while (0)
        const unsigned aev = wid >= 4 ? 0x54u : (0xc0u | (wid == 0 ? 0x24u : 0u) | ((wid == 1 && blockIdx.x == 0) ? 0x10u : 0u));
        int slot = 0;
        STAMP(2, 3);
        for (int st = 0; st < AT_NST; ++st) {
            const int nslot = (slot == 2) ? 0 : slot + 1;
            const lds_ptr base = lds + slot * AT_STAGE + kh * (2 * AT_KT * 64), nbase = lds + nslot * AT_STAGE + kh * (2 * AT_KT * 64);
            SB0();
            ATILE(kA, vB, kB, vA, base + 2048, base);
            ATILE(kB, vA, kA, vB, base + 4096, base + 2048);
            ATILE(kA, vB, kB, vA, base + 6144, base + 4096);
            if (__builtin_expect((aev >> st) & 1u, 0)) {
                if (wid < 4) {
                    if (attempt == 0 && st == 5 && wid == 0) poll_tags(flags, hwait + lane, T, 16);
                    if (attempt == 0 && st == 4) {
#pragma unroll 1
                        for (int i = 0; i < 4; ++i) poll_tags(flags, F_ST + 64 * i + lane, T, 64);
                        if (lane == 0) __hip_atomic_store(flags + F_E, T, RLX_AGENT); }
                    WAIT_BAR(0);
                    if (st + 2 < AT_NST) stage(st + 2, (slot == 0) ? 2 : slot - 1);
                } else {
                    WAIT_BAR(0);
                    if (attempt == 0 && st < 6) {
                        const int w = (blockIdx.x >> 3) * 4 + wid - 4; const bool second = st == 4;
                        const int n = g2.kh ? (second ? 8 : 10) : 9, k0 = g2.kh ? (second ? w * 8 : 2304 + w * 10) : w * 18 + (second ? 9 : 0);
                        const rsrc_t rp = mkrsrc(((g2.kh && second) ? g2.wo_ : g2.w2_) + (g2.cb >> 2) * 512, (DFF * D_ - 512) * 2);
                        for (int i = 0; i < n; ++i) bl_lds(rp, lds + JUNK, lane * 16, (k0 + i) * (D_ * 2)); } }
                if (attempt == 0 && st == 2 && hpub >= 0) publish(flags, hpub, T);
                if (attempt == 0 && st == AT_NST - 2) g2_stage(lds, g2, 0, 0);
            } else if (wid < 4) { WAIT_BAR(0); stage(st + 2, (slot == 0) ? 2 : slot - 1); }
            else LGKM_BAR();
            SB0();
            ATILE(kB, vA, kA, vB, nbase, base + 6144);
            slot = nslot;
        }
        o[1] = MFMA32(cat4(vB[0], vB[1]), PF(pb0), o[1]); o[1] = MFMA32(cat4(vB[2], vB[3]), PF(pb1), o[1]);
        ls[1] = MFMA16(sel, PF(pb0), ls[1]); ls[1] = MFMA16(sel, PF(pb1), ls[1]);
#undef ATILE
#undef ASTEP
#undef EXP2
#undef PF
        LGKM_BAR();
        STAMP(2, 4);
        if (attempt == 1) break;
        bool bad = false;
#pragma unroll
        for (int a = 0; a < 2; ++a) { bad |= !(ls[a][0] < 1.0e18f) || !(ls[a][1] < 1.0e18f);
#pragma unroll
            for (int r = 0; r < 16; ++r) bad |= !(fabsf(o[a][r]) < 1.0e30f); }
        LAS unsigned* fl = (LAS unsigned*)(lds + AT_FL);
        if (lane == 0) fl[wid] = __any(bad) ? 1u : 0u;
        LGKM_BAR();
        const u32x4 f0 = *(const LAS u32x4*)fl, f1 = *(const LAS u32x4*)(fl + 4);
        if ((f0[0] | f0[1] | f0[2] | f0[3] | f1[0] | f1[1] | f1[2] | f1[3]) == 0u) break;
        float mx[2] = {-INFINITY, -INFINITY};
        for (int st = 0; st < AT_NST; ++st) {
            if (wid < 4) stage(st, 0);
            WAIT_BAR(0);
#pragma unroll
            for (int h2 = 0; h2 < 2; ++h2)
                for (int kt = 0; kt < AT_KT / 32; ++kt) {
                    const lds_ptr base = lds + h2 * (2 * AT_KT * 64) + kt * 2048;
                    const bf16x8 k0 = lds_b128(base + kr[0]), k1 = lds_b128(base + kr[1]);
#pragma unroll
                    for (int a = 0; a < 2; ++a) { f32x16 s = {}; s = MFMA32(k0, qf[a][0], s); s = MFMA32(k1, qf[a][1], s);
#pragma unroll
                        for (int r = 0; r < 16; ++r) mx[a] = fmaxf(mx[a], s[r]); }
                }
            LGKM_BAR();
        }
#pragma unroll
        for (int a = 0; a < 2; ++a) { auto sw = __builtin_amdgcn_permlane32_swap(__float_as_uint(mx[a]), __float_as_uint(mx[a]), false, false);
            mref[a] = fmaxf(__uint_as_float(sw[0]), __uint_as_float(sw[1])); }
    }
    g2_stage(lds, g2, 1, 1);
    LAS float* xo = (LAS float*)lds + wq * (2 * 17 * 64);
    if (kh == 1) {
#pragma unroll
        for (int a = 0; a < 2; ++a) {
#pragma unroll
            for (int r = 0; r < 16; ++r) xo[(a * 17 + r) * 64 + lane] = o[a][r];
            if (lane < 16) { xo[(a * 17 + 16) * 64 + lane] = ls[a][0]; xo[(a * 17 + 16) * 64 + 16 + lane] = ls[a][1]; } }
    } else {
#pragma unroll
        for (int a = 0; a < 2; ++a) if (lane < 16) { xo[(a * 17 + 16) * 64 + 32 + lane] = ls[a][0]; xo[(a * 17 + 16) * 64 + 48 + lane] = ls[a][1]; }
    }
    LGKM_BAR();
    if (kh == 0) {
        const rsrc_t rsa = mkrsrc(ao, S_ * LDAO * 2);
#pragma unroll
        for (int a = 0; a < 2; ++a) {
            const float l = xo[(a * 17 + 16) * 64 + r32] + xo[(a * 17 + 16) * 64 + 32 + r32];
            const float rl = 1.0f / l;
            unsigned pk[8];
#pragma unroll
            for (int r = 0; r < 8; ++r) pk[r] = cvtpk((o[a][2 * r] + xo[(a * 17 + 2 * r) * 64 + lane]) * rl, (o[a][2 * r + 1] + xo[(a * 17 + 2 * r + 1) * 64 + lane]) * rl);
            const int dst = (qb * 256 + wq * 64 + a * 32 + r32) * LDAO + DFF + head * DH;
#pragma unroll
            for (int k = 0; k < 4; k += 2) {
                auto r0 = __builtin_amdgcn_permlane32_swap(pk[2 * k], pk[2 * k + 2], false, false); auto r1 = __builtin_amdgcn_permlane32_swap(pk[2 * k + 1], pk[2 * k + 3], false, false);
                st_wt((u32x4){r0[0], r1[0], r0[1], r1[1]}, rsa, (unsigned)(dst + 8 * k + 8 * hi) * 2u);
            }
        }
    }
    LGKM_BAR();
    STAMP(2, 5);
}

__device__ __forceinline__ void gemm2_body(lds_ptr lds, const G2& g2, const bf16_t* __restrict__ x, bf16_t* __restrict__ out, float* part, unsigned* flags, const unsigned T, const int rb, const int cb, const int kh, const int opub) {
    const int NKT = g2.nkt;
    const int tid = threadIdx.x, lane = tid & 63, wid = __builtin_amdgcn_readfirstlane(tid >> 6), kg = wid >> 2, wm = (wid >> 1) & 1, wn = wid & 1;
    const int fr = lane & 15, g = lane >> 4, q = fr >> 2, p = lane & 3;
    const int m0 = rb * 128, n0 = cb * 128;
    const int ar = (64 * wm + fr) * 128 + (((4 * kg + g) ^ (fr & 7)) * 16);
    int br[4];
    { const int swz = 2 * (q | ((g & 1) << 2));
#pragma unroll
      for (int j = 0; j < 4; ++j) br[j] = 16384 + (32 * kg + 8 * g + q) * 256 + (((2 * (4 * wn + j) + (p >> 1)) ^ swz) * 16) + (p & 1) * 8; }
    f32x4 acc[4][4];
#pragma unroll
    for (int i = 0; i < 4; ++i)
#pragma unroll
        for (int j = 0; j < 4; ++j) acc[i][j] = (f32x4){0.f, 0.f, 0.f, 0.f};
    bf16x8 A0[4], A1[4]; s16x4 B0[4][2], B1[4][2];
    const int wid4 = wid; const rsrc_t rA2 = g2.rA(), rW2 = g2.rW2(), rWo = g2.rWo();
    auto st1 = [&](int t, int sl, int i) {
        const int gt = g2.k0 + t; const lds_ptr d = lds + g2_slot(sl);
        if (i < 2) bl_lds_dev(rA2, d + (16 * wid4 + 8 * i) * 128, g2.aoff, t * 128 + i * (8 * LDAO * 2));
        else { const bool w = gt < 56; bl_lds(w ? rW2 : rWo, d + 16384 + (8 * wid4 + 4 * (i - 2)) * 256, g2.boff[i - 2], (w ? gt : gt - 56) * (64 * D_ * 2)); } };
#define RDONE(AN, BN_, so, r) do { if ((r) < 4) AN[(r) & 3] = lds_b128(lds + (so) + ar + ((r) & 3) * 2048); \
        else if ((r) < 12) BN_[(((r) - 4) >> 1) & 3][(r) & 1] = lds_tr(lds + (so) + br[(((r) - 4) >> 1) & 3] + ((r) & 1) * 1024); } while (0)
#define STEP(AC, BC, AN, BN_, so, tl, sl) do { \
        unroll<0, 16>([&](auto ic) { constexpr int idx = decltype(ic)::value; constexpr int j = idx >> 2, i = idx & 3; \
            acc[i][j] = MFMA16(cat4(BC[j][0], BC[j][1]), AC[i], acc[i][j]); SB0(); \
            RDONE(AN, BN_, so, idx); \
            if ((idx & 3) == 2) st1(tl, sl, idx >> 2); \
            SB0(); }); } while (0)
    STAMP(3, 0);
    g2_stage(lds, g2, 2, 2); g2_stage(lds, g2, 3, 3);
    WAIT_BAR(8);
    publish(flags, opub, T);
    STAMP(3, 1);
    unroll<0, 12>([&](auto ic) { constexpr int idx = decltype(ic)::value; RDONE(A0, B0, g2_slot(0), idx); });
    WAIT_BAR(8);
    for (int t = 0; t < NKT; t += 2) {
        if (kh == 1 && t == 48 - g2.k0 && wid == 0) poll_tags(flags, F_O + (rb >> 1) * 32 + (lane & 31), T, 32);
        SB0();
        STEP(A0, B0, A1, B1, g2_slot(t + 1), min(t + 4, NKT - 1), t);
        WAIT_BAR(8); SB0();
        STEP(A1, B1, A0, B0, g2_slot(t + 2), min(t + 5, NKT - 1), t + 1);
        WAIT_BAR(8);
    }
#undef RDONE
#undef STEP
    WAIT_BAR(0);
    STAMP(3, 2);
    const int mrow = m0 + 64 * wm + 32 * kg + 16 * kh + fr;
    u32x2 xv[4];
#pragma unroll
    for (int j = 0; j < 4; ++j) xv[j] = *(const u32x2*)(x + (size_t)mrow * D_ + n0 + 64 * wn + 16 * j + 4 * g);
    LAS f32x4* xr = (LAS f32x4*)lds + (wid & 3) * 1024;
#pragma unroll
    for (int i = 0; i < 2; ++i)
#pragma unroll
        for (int j = 0; j < 4; ++j) xr[(kg * 8 + i * 4 + j) * 64 + lane] = kg ? acc[i][j] : acc[2 + i][j];
    LGKM_BAR();
    f32x4 r[2][4];
#pragma unroll
    for (int i = 0; i < 2; ++i)
#pragma unroll
        for (int j = 0; j < 4; ++j) r[i][j] = (kg ? acc[2 + i][j] : acc[i][j]) + xr[((kg ^ 1) * 8 + i * 4 + j) * 64 + lane];
    const int tile = rb * 8 + cb;
    { const rsrc_t rs = mkrsrc(part + (size_t)(tile * 2 + kh) * 8192, 32768);
#pragma unroll
      for (int j = 0; j < 4; ++j) st_wt(__builtin_bit_cast(u32x4, kh ? r[0][j] : r[1][j]), rs, (wid * 4 + j) * 1024 + lane * 16); }
    asm volatile("s_waitcnt vmcnt(0)" ::: "memory");
    if (lane == 0) __hip_atomic_store(flags + F_P + (tile * 2 + kh) * 32 + wid, T, RLX_AGENT);
    poll_tags(flags, F_P + (tile * 2 + (kh ^ 1)) * 32 + wid, T, 1);
    STAMP(3, 3);
    { const rsrc_t rs = mkrsrc(part + (size_t)(tile * 2 + (kh ^ 1)) * 8192, 32768);
      f32x4 o[4];
#pragma unroll
      for (int j = 0; j < 4; ++j) o[j] = __builtin_bit_cast(f32x4, __builtin_amdgcn_raw_buffer_load_b128(rs, (wid * 4 + j) * 1024 + lane * 16, 0, 16));
      unsigned pk[4][2];
#pragma unroll
      for (int j = 0; j < 4; ++j) { f32x4 v = (kh ? r[1][j] : r[0][j]) + o[j];
          v[0] += __uint_as_float(xv[j][0] << 16); v[1] += __uint_as_float(xv[j][0] & 0xffff0000u); v[2] += __uint_as_float(xv[j][1] << 16); v[3] += __uint_as_float(xv[j][1] & 0xffff0000u);
          pk[j][0] = cvtpk(v[0], v[1]); pk[j][1] = cvtpk(v[2], v[3]); }
#pragma unroll
      for (int j = 0; j < 4; j += 2) {
          auto r0 = __builtin_amdgcn_permlane16_swap(pk[j][0], pk[j + 1][0], false, false); auto r1 = __builtin_amdgcn_permlane16_swap(pk[j][1], pk[j + 1][1], false, false);
          *(u32x4*)(out + (size_t)mrow * D_ + n0 + 64 * wn + 16 * (j + (g & 1)) + 8 * (g >> 1)) = (u32x4){r0[0], r1[0], r0[1], r1[1]}; } }
    STAMP(3, 4);
}

__global__ __launch_bounds__(512, 2) void fused_kernel(const bf16_t* __restrict__ wqkv, const bf16_t* __restrict__ w1, const bf16_t* __restrict__ w2, const bf16_t* __restrict__ wo,
                                                       const bf16_t* __restrict__ x, bf16_t* qkv, bf16_t* ao, bf16_t* __restrict__ out, float* part, unsigned* flags) {
    __shared__ __attribute__((aligned(16))) char smem[LDS_BYTES];
    const lds_ptr lds = (lds_ptr)smem;
    const int xg = blockIdx.x & 7, r = blockIdx.x >> 3;
    const int rb1 = (xg & 1) * 4 + (r & 3), c1 = (xg >> 1) * 4 + ((r >> 2) & 3), ffn = r >> 4;
    unsigned T;
    if (ffn) T = gemm1_body<7>(lds, x, w1, DFF, rb1 * 256, c1 * 224, qkv, ao, flags, 16 + c1, wqkv, w1);
    else { T = gemm1_body<6>(lds, x, wqkv, 3 * D_, rb1 * 256, c1 * 192, qkv, ao, flags, c1, wqkv, w1); publish(flags, F_Q + c1 * 32 + rb1, T); }
    const int kh = xg & 1, rr = kh ? (r ^ 16) : r, rb3 = ((xg >> 1) & 1) * 8 + (rr & 7), cb3 = (xg >> 2) * 4 + (rr >> 3);
    const G2 g2 = g2_setup(ao, w2, wo, rb3, cb3, kh, (rr >> 4) ? 72 - SPLITQ : SPLITQ);
    const int head = xg * 4 + (r >> 3), qb = r & 7;
    attn_body(lds, qkv, ao, flags, T, head, qb, ffn ? F_H + rb1 * 32 + c1 : -1, F_H + (rb3 >> 1) * 32, g2);
    gemm2_body(lds, g2, x, out, part, flags, T, rb3, cb3, kh, F_O + qb * 32 + head);
}

extern "C" void kernel_launch(void* const* d_in, const int* in_sizes, int n_in, void* d_out, int out_size, void* d_ws, size_t ws_size, hipStream_t stream) {
    const bf16_t* x = (const bf16_t*)d_in[0]; const bf16_t* wqkv = (const bf16_t*)d_in[1]; const bf16_t* wo = (const bf16_t*)d_in[2];
    const bf16_t* w1 = (const bf16_t*)d_in[4]; const bf16_t* w2 = (const bf16_t*)d_in[5];
    char* ws = (char*)d_ws;
    unsigned* flags = (unsigned*)(ws + WS_FLAGS); bf16_t* qkv = (bf16_t*)(ws + WS_QKV); bf16_t* ao = (bf16_t*)(ws + WS_AO); float* part = (float*)(ws + WS_PART);
    fused_kernel<<<256, 512, 0, stream>>>(wqkv, w1, w2, wo, x, qkv, ao, (bf16_t*)d_out, part, flags);
}
```

```cpp
#include <hip/hip_runtime.h>
#include <hip/hip_bf16.h>
#include <stdint.h>

typedef unsigned short bf16_t;
typedef short bf16x8 __attribute__((ext_vector_type(8)));
typedef short s16x4 __attribute__((ext_vector_type(4)));
typedef float f32x2 __attribute__((ext_vector_type(2)));
typedef float f32x4 __attribute__((ext_vector_type(4)));
typedef float f32x16 __attribute__((ext_vector_type(16)));
typedef unsigned u32x4 __attribute__((ext_vector_type(4)));
typedef unsigned u32x2 __attribute__((ext_vector_type(2)));
typedef __bf16 bf16x2n __attribute__((ext_vector_type(2)));
typedef __bf16 bf16x4n __attribute__((ext_vector_type(4)));
#define LAS __attribute__((address_space(3)))
typedef LAS char* lds_ptr;

constexpr int S_ = 2048, D_ = 1024, H_ = 32, DH = 32, DFF = 3584;
constexpr int LDAO = DFF + D_;
constexpr size_t WS_FLAGS = 0;
constexpr size_t WS_QKV = 1u << 20;
constexpr size_t WS_AO = WS_QKV + (size_t)3 * S_ * D_ * 2;
constexpr size_t WS_PART = WS_AO + (size_t)S_ * LDAO * 2;
constexpr int LDS_BYTES = 163840;
constexpr int F_E = 0, F_ST = 256, F_Q = 1024, F_H = 2048, F_O = 3072, F_P = 4096;
constexpr int F_LT = 512, F_CT = 768;
constexpr int F_LN = 16384, F_CS = 32768;
constexpr int NCOL = 3 * D_ + DFF;
#define SPLITQ 40
constexpr int TBL = 151552;

#define EXP_NOADD 0
#define EXP_NOROT 0
#define EXP_NOHST 0
#define STAMP(k, i) do {} while (0)

__device__ __forceinline__ unsigned cvtpk(float lo, float hi) { return __builtin_bit_cast(unsigned, __builtin_convertvector((f32x2){lo, hi}, bf16x2n)); }
__device__ __forceinline__ float rbf(float x) { return __uint_as_float(cvtpk(x, x) << 16); }
typedef __amdgpu_buffer_rsrc_t rsrc_t;
__device__ __forceinline__ rsrc_t mkrsrc(const void* p, unsigned bytes) { return __builtin_amdgcn_make_buffer_rsrc((void*)p, 0, bytes, 0x00020000); }
__device__ __forceinline__ void bl_lds(rsrc_t r, lds_ptr l, unsigned voff, unsigned soff) {
    asm volatile("s_mov_b32 m0, %0\n\ts_nop 0\n\tbuffer_load_dwordx4 %1, %2, %3 offen lds" :: "s"(__builtin_amdgcn_readfirstlane((unsigned)(uintptr_t)l)), "v"(voff), "s"(r), "s"(__builtin_amdgcn_readfirstlane(soff)) : "memory"); }
__device__ __forceinline__ void bl_lds_dev(rsrc_t r, lds_ptr l, unsigned voff, unsigned soff) {
    asm volatile("s_mov_b32 m0, %0\n\ts_nop 0\n\tbuffer_load_dwordx4 %1, %2, %3 offen sc1 lds" :: "s"(__builtin_amdgcn_readfirstlane((unsigned)(uintptr_t)l)), "v"(voff), "s"(r), "s"(__builtin_amdgcn_readfirstlane(soff)) : "memory"); }
__device__ __forceinline__ void st_wt(u32x4 v, rsrc_t r, unsigned off) { __builtin_amdgcn_raw_buffer_store_b128(v, r, off, 0, 16); }
__device__ __forceinline__ bf16x8 lds_b128(lds_ptr p) { return *(const LAS bf16x8*)p; }
__device__ __forceinline__ s16x4 lds_tr(lds_ptr p) { return __builtin_amdgcn_ds_read_tr16_b64_v4i16((LAS s16x4*)p); }
__device__ __forceinline__ bf16x8 cat4(s16x4 a, s16x4 b) { return (bf16x8){a[0], a[1], a[2], a[3], b[0], b[1], b[2], b[3]}; }
#define MFMA16(a, b, c) __builtin_amdgcn_mfma_f32_16x16x32_bf16(a, b, c, 0, 0, 0)
#define MFMA32(a, b, c) __builtin_amdgcn_mfma_f32_32x32x16_bf16(a, b, c, 0, 0, 0)
#define WAIT_BAR(N) asm volatile("s_waitcnt vmcnt(%0) lgkmcnt(0)\n\ts_barrier" :: "n"(N) : "memory")
#define LGKM_BAR() asm volatile("s_waitcnt lgkmcnt(0)\n\ts_barrier" ::: "memory")
#define SB0() __builtin_amdgcn_sched_barrier(0)
#define RLX_AGENT __ATOMIC_RELAXED, __HIP_MEMORY_SCOPE_AGENT
template <int I> struct IC { static constexpr int value = I; };
template <int B, int E, class F> __device__ __forceinline__ void unroll(F&& f) { if constexpr (B < E) { f(IC<B>{}); unroll<B + 1, E>(f); } }

__device__ __forceinline__ float wave_sum(float v) {
    v += __builtin_bit_cast(float, __builtin_amdgcn_update_dpp(0, __builtin_bit_cast(int, v), 0xB1, 0xf, 0xf, true));
    v += __builtin_bit_cast(float, __builtin_amdgcn_update_dpp(0, __builtin_bit_cast(int, v), 0x4E, 0xf, 0xf, true));
    v += __builtin_bit_cast(float, __builtin_amdgcn_update_dpp(0, __builtin_bit_cast(int, v), 0x141, 0xf, 0xf, true));
    v += __builtin_bit_cast(float, __builtin_amdgcn_update_dpp(0, __builtin_bit_cast(int, v), 0x140, 0xf, 0xf, true));
    { auto r = __builtin_amdgcn_permlane16_swap(__float_as_uint(v), __float_as_uint(v), false, false); v = __uint_as_float(r[0]) + __uint_as_float(r[1]); }
    { auto r = __builtin_amdgcn_permlane32_swap(__float_as_uint(v), __float_as_uint(v), false, false); v = __uint_as_float(r[0]) + __uint_as_float(r[1]); }
    return v;
}
__device__ __forceinline__ void poll_tags(unsigned* flags, int word, unsigned T, int n) {
    const bool act = (int)(threadIdx.x & 63) < n; unsigned spins = 0;
    for (;;) { unsigned v = T; if (act) v = __hip_atomic_load(flags + word, RLX_AGENT);
        if (__all(v == T)) break; __builtin_amdgcn_s_sleep(2); if (++spins > (1u << 22)) break; }
    asm volatile("" ::: "memory");
}
__device__ __forceinline__ void publish(unsigned* flags, int word, unsigned T) {
    if (threadIdx.x == 0) __hip_atomic_store(flags + word, T, RLX_AGENT);
}
typedef unsigned long long u64;
__device__ __forceinline__ void put_granule(unsigned* flags, int word, float v, unsigned T) { __hip_atomic_store((u64*)(flags + word), ((u64)T << 32) | __float_as_uint(v), RLX_AGENT); }
__device__ __forceinline__ u64 get_granule(unsigned* flags, int word) { return __hip_atomic_load((u64*)(flags + word), RLX_AGENT); }

constexpr int JUNK = 162816;
constexpr int HSTG = 65536;
template <int NT>
__device__ __forceinline__ unsigned gemm1_body(lds_ptr lds, const bf16_t* xn, const bf16_t* W, const int ldw, const int m0, const int n0, bf16_t* qkv, bf16_t* ao, unsigned* flags, const int j32, const bf16_t* wq_, const bf16_t* w1_) {
    constexpr int BN = 32 * NT, RB = BN * 2, CPR = BN / 8, BSLOT = 64 * RB, NKT = D_ / 64, GB = (NT == 7) ? 4 : 3;
    constexpr int LB = 65536, NMF = 4 * NT, NRD = 4 + 2 * NT, NBB = (NT == 7) ? 2 : 4;
    const int tid = threadIdx.x, lane = tid & 63, wid = __builtin_amdgcn_readfirstlane(tid >> 6), wm = wid & 3, wn = wid >> 2;
    const int fr = lane & 15, g = lane >> 4, q = fr >> 2, p = lane & 3;
    const rsrc_t rA = mkrsrc(xn + (size_t)m0 * D_, 256 * D_ * 2), rB = mkrsrc(W + n0, (unsigned)(D_ * ldw - n0) * 2u);
    const unsigned aoff = (unsigned)((32 * wid + (lane >> 3)) * D_ + (((lane & 7) ^ ((lane >> 3) & 7)) * 8)) * 2u;
    unsigned boff[GB];
#pragma unroll
    for (int i = 0; i < GB; ++i) { const int idx = 64 * i + lane, rl = idx / CPR, cp = idx % CPR, k = (8 * wid + rl) & 63;
        const int swz = (NT == 7) ? 2 * ((k >> 3) & 1) : 2 * (((k >> 1) & 1) | (((k >> 3) & 1) << 1));
        boff[i] = (unsigned)(k * ldw + (cp ^ swz) * 8) * 2u; }
    const int krot = (m0 >> 10) * 8, tcs = ((m0 >> 8) & 3) * 2;
    auto stageA1 = [&](int kt_, int slot, int i) { const int kt = (kt_ + krot) & (NKT - 1); bl_lds(rA, lds + slot * 32768 + (32 * wid + 8 * i) * 128, aoff, kt * 128 + i * (8 * D_ * 2)); };
    auto stageB1 = [&](int kt_, int slot, int i) { const int kt = (kt_ + krot) & (NKT - 1); const unsigned so = (unsigned)(kt * 64 * ldw) * 2u;
        if (NT == 7 && i == 3) { if (lane < 32) bl_lds(rB, lds + LB + slot * BSLOT + (8 * wid * CPR + 192) * 16, boff[GB - 1], so); }
        else bl_lds(rB, lds + LB + slot * BSLOT + (8 * wid * CPR + 64 * i) * 16, boff[i < GB ? i : 0], so); };
    int ar[2];
#pragma unroll
    for (int ks = 0; ks < 2; ++ks) ar[ks] = (64 * wm + fr) * 128 + (((4 * ks + g) ^ (fr & 7)) * 16);
    int br[NBB];
    { const int swz = (NT == 7) ? 2 * (g & 1) : 2 * ((q >> 1) | ((g & 1) << 1));
#pragma unroll
      for (int j = 0; j < NBB; ++j) br[j] = LB + (8 * g + q) * RB + (((2 * (NT * wn + j) + (p >> 1)) ^ swz) * 16) + (p & 1) * 8; }
    f32x4 acc[4][NT];
#pragma unroll
    for (int i = 0; i < 4; ++i)
#pragma unroll
        for (int j = 0; j < NT; ++j) acc[i][j] = (f32x4){0.f, 0.f, 0.f, 0.f};
    bf16x8 A0[4], A1[4]; s16x4 B0[NT][2], B1[NT][2];
#define RDONE(AN, BN_, sa, sb, ks, r) do { if ((r) < 4) AN[(r) < 4 ? (r) : 0] = lds_b128(lds + (sa) + ar[ks] + ((r) < 4 ? (r) : 0) * 2048); \
        else if ((r) < NRD) { constexpr int jj = (((r) - 4) >> 1) < NT && (r) >= 4 ? (((r) - 4) >> 1) : 0, tt = ((r) - 4) & 1; BN_[jj][tt] = lds_tr(lds + (sb) + br[jj % NBB] + 32 * (jj - jj % NBB) + (32 * (ks) + 4 * tt) * RB); } } while (0)
#define SUBSTEP(AC, BC, AN, BN_, sa, sb, ks, STG, go, tl, sl) do { \
        unroll<0, NMF>([&](auto ic) { constexpr int idx = decltype(ic)::value; constexpr int j = idx >> 2, i = idx & 3; \
            acc[i][j] = MFMA16(cat4(BC[j][0], BC[j][1]), AC[i], acc[i][j]); SB0(); \
            RDONE(AN, BN_, sa, sb, ks, idx); \
            if ((idx % NT) == 1 && (go)) { if (STG == 1) stageA1(tl, sl, idx / NT); if (STG == 2 && idx / NT < GB) stageB1(tl, sl, idx / NT); } \
            SB0(); }); } while (0)
    STAMP(1, 0);
#pragma unroll
    for (int t = 0; t < 2; ++t) {
#pragma unroll
        for (int i = 0; i < GB; ++i) stageB1(t, t, i);
#pragma unroll
        for (int i = 0; i < 4; ++i) stageA1(t, t, i); }
    unsigned T; float lnv;
    const int ncg = (NT == 7 ? 3 * D_ : 0) + n0;
    {   const int row = m0 + 8 * j32 + wid;
        const u32x4* src = (const u32x4*)(xn + (size_t)row * D_);
        u32x4 v0, v1; unsigned ev;
        asm volatile("global_load_dwordx4 %0, %3, off\n\tglobal_load_dwordx4 %1, %3, off offset:1024\n\tglobal_load_dword %2, %4, off sc1"
                     : "=&v"(v0), "=&v"(v1), "=&v"(ev) : "v"(src + lane), "v"(flags + F_E) : "memory");
        asm volatile("s_waitcnt vmcnt(0)" : "+v"(v0), "+v"(v1), "+v"(ev) :: "memory");
        SB0();
        float f[16];
#pragma unroll
        for (int i = 0; i < 4; ++i) { f[2 * i] = __uint_as_float(v0[i] << 16); f[2 * i + 1] = __uint_as_float(v0[i] & 0xffff0000u); f[8 + 2 * i] = __uint_as_float(v1[i] << 16); f[8 + 2 * i + 1] = __uint_as_float(v1[i] & 0xffff0000u); }
        float sm = 0.f;
#pragma unroll
        for (int i = 0; i < 16; ++i) sm += f[i];
        const float mean = wave_sum(sm) * (1.0f / D_);
        float qq = 0.f;
#pragma unroll
        for (int i = 0; i < 16; ++i) { const float d = f[i] - mean; qq += d * d; }
        qq = wave_sum(qq);
        T = (unsigned)__builtin_amdgcn_readfirstlane(ev) + 1u;
        const float sd = rbf(__fsqrt_rn(rbf(rbf(qq * (1.0f / D_)) + 1e-5f)));
        lnv = (lane & 1) ? 1.0f / sd : rbf(mean);
    }
    WAIT_BAR(0);
    auto colsum = [&](s16x4 (&BC)[NT][2]) {
        const bf16x8 ones = {0x3f80, 0x3f80, 0x3f80, 0x3f80, 0x3f80, 0x3f80, 0x3f80, 0x3f80}; const f32x4 z = {0.f, 0.f, 0.f, 0.f};
        float c[NT];
#pragma unroll
        for (int j = 0; j < NT; ++j) c[j] = MFMA16(ones, cat4(BC[j][0], BC[j][1]), z)[0];
        if (lane < 16) {
#pragma unroll
            for (int j = 0; j < NT; ++j) ((LAS float*)(lds + TBL + 2048))[wm * 256 + 16 * (NT * wn + j) + lane] = c[j]; } };
    STAMP(1, 1);
    unroll<0, NRD>([&](auto ic) { constexpr int idx = decltype(ic)::value; RDONE(A0, B0, 0, 0, 0, idx); });
    int sa = 0, sb = 0, sb3 = 0;
    const rsrc_t rF = mkrsrc(flags, 1u << 20);
    unsigned long long itv = 0;
    const unsigned evmask = 1u | (1u << (tcs + (wm >> 1))) | (1u << (tcs + 3)) | (1u << (NKT - 2)) | (wid == 0 ? (4u | (1u << (tcs + 5)) | (1u << (NKT - 3))) : 0u);
    for (int t = 0; t < NKT; ++t) {
        const int tn = min(t + 2, NKT - 1); constexpr bool go = true;
        SB0();
        { const int slb = (sb3 == 0) ? 2 : sb3 - 1;
          SUBSTEP(A0, B0, A1, B1, sa, sb, 1, 2, go, tn, slb); }
        if (__builtin_expect((evmask >> t) & 1u, 0)) {
            if (t == 0) { if (lane < 2) __hip_atomic_store(flags + F_LN + (m0 + 8 * j32 + wid) * 2 + lane, __float_as_uint(lnv), RLX_AGENT);
                if (wid == 0) { publish(flags, F_ST + blockIdx.x, T); WAIT_BAR(GB + 2); } else WAIT_BAR(GB + 1); }
            else if (wid == 0 && (t == 2 || t == tcs + 5)) { publish(flags, (t == 2 ? F_LT : F_CT) + (m0 >> 8) * 32 + j32, T); WAIT_BAR(GB + 1); }
            else if (wid * 64 < BN && t == tcs + 3) { if (tid < BN) { const LAS float* pc = (const LAS float*)(lds + TBL + 2048) + tid;
                    __hip_atomic_store(flags + F_CS + (m0 >> 8) * NCOL + ncg + tid, __float_as_uint((pc[0] + pc[256]) + (pc[512] + pc[768])), RLX_AGENT); }
                WAIT_BAR(GB + 1); }
            else WAIT_BAR(GB);
            if (t == tcs + (wm >> 1)) { if (wm & 1) colsum(B1); else colsum(B0); }
            if (t == NKT - 3 && wid == 0) poll_tags(flags, lane < 32 ? F_LT + (m0 >> 8) * 32 + lane : F_CT + (lane & 7) * 32 + j32, T, 40);
            if (t == NKT - 2) {
                if (lane < BN / 4) bl_lds_dev(rF, lds + TBL + 2048 + wid * 1024, lane * 16, (F_CS + wid * NCOL + ncg) * 4);
                if (wid < 2) bl_lds_dev(rF, lds + TBL + wid * 1024, lane * 16, (F_LN + m0 * 2) * 4 + wid * 1024); }
        } else WAIT_BAR(GB);
        SB0();
        sa ^= 32768; sb3 = (sb3 == 2) ? 0 : sb3 + 1; sb = sb3 * BSLOT;
        SUBSTEP(A1, B1, A0, B0, sa, sb, 0, 1, go, tn, t & 1);
    }
#undef RDONE
#undef SUBSTEP
    STAMP(1, 2);
    if (tid < BN) { float cs = 0.f;
#pragma unroll
        for (int i = 0; i < 8; ++i) cs += ((const LAS float*)(lds + TBL + 2048))[i * 256 + tid];
        ((LAS float*)(lds + TBL + 10240))[tid] = cs; }
    WAIT_BAR(0);
#pragma unroll
    for (int i = 0; i < 4; ++i) { const f32x2 st = ((const LAS f32x2*)(lds + TBL))[64 * wm + 16 * i + fr];
#pragma unroll
        for (int j = 0; j < NT; ++j) { const f32x4 cs = *(const LAS f32x4*)(lds + TBL + 10240 + (16 * (NT * wn + j) + 4 * g) * 4); acc[i][j] = (acc[i][j] - cs * st[0]) * st[1]; } }
    const int mrow = m0 + 64 * wm + fr;
    if constexpr (NT == 6) {
        const rsrc_t rsq = mkrsrc(qkv, 3 * S_ * D_ * 2);
#pragma unroll
        for (int b = 0; b < 3; ++b) {
            const int col = n0 + 96 * wn + 32 * b, which = col >> 10, head = (col & 1023) >> 5;
            const float sc = (which == 0) ? 0.17677669529663687f * 1.4426950408889634f : 1.0f;
            const int dst = ((which * H_ + head) * S_) * DH;
#pragma unroll
            for (int i = 0; i < 4; ++i) {
                const f32x4 u = acc[i][2 * b] * sc, v = acc[i][2 * b + 1] * sc;
                unsigned x0 = cvtpk(u[0], u[1]), x1 = cvtpk(u[2], u[3]), y0 = cvtpk(v[0], v[1]), y1 = cvtpk(v[2], v[3]);
                auto r0 = __builtin_amdgcn_permlane16_swap(x0, y0, false, false); auto r1 = __builtin_amdgcn_permlane16_swap(x1, y1, false, false);
                const int tok = mrow + 16 * i; int ch = 2 * (g & 1) + (g >> 1); if (which == 1) ch ^= (tok >> 2) & 3;
                st_wt((u32x4){r0[0], r1[0], r0[1], r1[1]}, rsq, (unsigned)(dst + tok * DH + ch * 8) * 2u);
            }
        }
        WAIT_BAR(0);
    } else {
        const rsrc_t rsa = mkrsrc(ao, S_ * LDAO * 2);
        u32x4 sv[14];
        auto pk = [&](const f32x4& a, const f32x4& b) { const f32x4 z = {0.f, 0.f, 0.f, 0.f}; const f32x4 u = __builtin_elementwise_max(a, z), v = __builtin_elementwise_max(b, z);
            unsigned x0 = cvtpk(u[0], u[1]), x1 = cvtpk(u[2], u[3]), y0 = cvtpk(v[0], v[1]), y1 = cvtpk(v[2], v[3]);
            auto r0 = __builtin_amdgcn_permlane16_swap(x0, y0, false, false); auto r1 = __builtin_amdgcn_permlane16_swap(x1, y1, false, false); return (u32x4){r0[0], r1[0], r0[1], r1[1]}; };
        auto off = [&](int s, int half) -> unsigned {
            const int c0 = n0 + 112 * half;
            if (s < 12) return (unsigned)((mrow + 16 * (s & 3)) * LDAO + c0 + 16 * half + 32 * (s >> 2) + 16 * (g & 1) + 8 * (g >> 1)) * 2u;
            return (unsigned)((mrow + 16 * (2 * (s - 12) + (g & 1))) * LDAO + c0 + (half ? 0 : 96) + 8 * (g >> 1)) * 2u; };
        if (wn == 0) {
#pragma unroll
            for (int b = 0; b < 3; ++b)
#pragma unroll
                for (int i = 0; i < 4; ++i) sv[4 * b + i] = pk(acc[i][2 * b], acc[i][2 * b + 1]);
#pragma unroll
            for (int i = 0; i < 2; ++i) sv[12 + i] = pk(acc[2 * i][6], acc[2 * i + 1][6]);
#pragma unroll
            for (int s = 0; s < 14; ++s) *(LAS u32x4*)(lds + HSTG + (wm * 14 + s) * 1024 + lane * 16) = sv[s];
            LGKM_BAR();
        } else {
#pragma unroll
            for (int b = 0; b < 3; ++b)
#pragma unroll
                for (int i = 0; i < 4; ++i) sv[4 * b + i] = pk(acc[i][2 * b + 1], acc[i][2 * b + 2]);
#pragma unroll
            for (int i = 0; i < 2; ++i) sv[12 + i] = pk(acc[2 * i][0], acc[2 * i + 1][0]);
#pragma unroll
            for (int s = 0; s < 14; ++s) if (!EXP_NOHST) st_wt(sv[s], rsa, off(s, 1));
            LGKM_BAR();
#pragma unroll
            for (int s = 0; s < 14; ++s) sv[s] = *(const LAS u32x4*)(lds + HSTG + (wm * 14 + s) * 1024 + lane * 16);
#pragma unroll
            for (int s = 0; s < 14; ++s) if (!EXP_NOHST || sv[s][0] == 0x12345678) st_wt(sv[s], rsa, off(s, 0));
        }
    }
    STAMP(1, 3);
    return T;
}

struct G2 { const bf16_t *ao_, *w2_, *wo_; unsigned aoff, boff[2]; int rb, cb, kh, k0, nkt;
    __device__ __forceinline__ rsrc_t rA() const { return mkrsrc(ao_ + (size_t)rb * 128 * LDAO + k0 * 64, (unsigned)(128 * LDAO - k0 * 64) * 2u); }
    __device__ __forceinline__ rsrc_t rW2() const { return mkrsrc(w2_ + cb * 128, (unsigned)(DFF * D_ - cb * 128) * 2u); }
    __device__ __forceinline__ rsrc_t rWo() const { return mkrsrc(wo_ + cb * 128, (unsigned)(D_ * D_ - cb * 128) * 2u); } };
__device__ __forceinline__ G2 g2_setup(const bf16_t* ao, const bf16_t* w2, const bf16_t* wo, int rb, int cb, int kh, int split) {
    const int lane = threadIdx.x & 63, wid = __builtin_amdgcn_readfirstlane(threadIdx.x >> 6);
    G2 s; s.kh = kh; s.k0 = kh ? split : 0; s.nkt = kh ? 72 - split : split; s.rb = rb; s.cb = cb; s.ao_ = ao; s.w2_ = w2; s.wo_ = wo;
    s.aoff = (unsigned)((16 * wid + (lane >> 3)) * LDAO + (((lane & 7) ^ ((lane >> 3) & 7)) * 8)) * 2u;
#pragma unroll
    for (int i = 0; i < 2; ++i) { const int k = 8 * wid + 4 * i + (lane >> 4), cp = lane & 15, swz = 2 * ((k & 3) | (((k >> 3) & 1) << 2)); s.boff[i] = (unsigned)(k * D_ + (cp ^ swz) * 8) * 2u; }
    return s;
}
__device__ __forceinline__ int g2_slot(int t) { return (int)((0x10002030u >> (8 * (t & 3))) & 0xffu) * 2048; }
__device__ __forceinline__ void g2_stage(lds_ptr lds, const G2& s, int t, int sl) {
    const int wid = __builtin_amdgcn_readfirstlane(threadIdx.x >> 6), gt = s.k0 + t; const lds_ptr d = lds + g2_slot(sl);
#pragma unroll
    for (int i = 0; i < 2; ++i) bl_lds_dev(s.rA(), d + (16 * wid + 8 * i) * 128, s.aoff, t * 128 + i * (8 * LDAO * 2));
    if (gt < 56) {
#pragma unroll
        for (int i = 0; i < 2; ++i) bl_lds(s.rW2(), d + 16384 + (8 * wid + 4 * i) * 256, s.boff[i], gt * (64 * D_ * 2));
    } else {
#pragma unroll
        for (int i = 0; i < 2; ++i) bl_lds(s.rWo(), d + 16384 + (8 * wid + 4 * i) * 256, s.boff[i], (gt - 56) * (64 * D_ * 2));
    }
}

constexpr int AT_KT = 128, AT_STAGE = 4 * AT_KT * 64, AT_NST = 1024 / AT_KT, AT_Q = 122880, AT_FL = 139264;
__device__ __forceinline__ void attn_body(lds_ptr lds, const bf16_t* qkv, bf16_t* ao, unsigned* flags, const unsigned T, const int head, const int qb, const int hpub, const int hwait, const G2& g2) {
    const int tid = threadIdx.x, lane = tid & 63, wid = __builtin_amdgcn_readfirstlane(tid >> 6), wq = wid & 3, kh = wid >> 2;
    const int r32 = lane & 31, hi = lane >> 5;
    const rsrc_t rs = mkrsrc(qkv, 3 * S_ * D_ * 2);
    const unsigned ssrc = (unsigned)((((1 + (wid & 1)) * H_ + head) * S_ + (wid >> 1) * 1024) * 64);
    auto stage = [&](int st, int slot) {
#pragma unroll
        for (int i = 0; i < 8; ++i) bl_lds_dev(rs, lds + slot * AT_STAGE + (wid & 3) * 8192 + i * 1024, lane * 16, ssrc + st * (AT_KT * 64) + i * 1024); };
    STAMP(2, 0);
    if (wid < 4) {
        poll_tags(flags, F_Q + (((lane >> 3) & 3) * 1024 + head * 32) / 192 * 32 + (lane & 7), T, 24);
        STAMP(2, 1);
#pragma unroll
        for (int i = 0; i < 4; ++i) bl_lds_dev(rs, lds + AT_Q + wid * 4096 + i * 1024, lane * 16, (unsigned)((head * S_ + qb * 256) * 64 + wid * 4096 + i * 1024));
    }
    const int kr[2] = {r32 * 64 + ((hi ^ ((r32 >> 2) & 3)) * 16), r32 * 64 + (((2 + hi) ^ ((r32 >> 2) & 3)) * 16)};
    const int vr = AT_KT * 64 + (4 * hi + ((lane & 15) >> 2)) * 64 + ((lane >> 4) & 1) * 32 + (lane & 3) * 8;
    bf16x8 sel;
    { const short one = ((lane & 15) == ((lane >> 4) & 1)) ? (short)0x3f80 : (short)0;
#pragma unroll
      for (int j = 0; j < 8; ++j) sel[j] = one; }
    bf16x8 qf[2][2];
    float mref[2];
    f32x16 o[2]; f32x4 ls[2];
    for (int attempt = 0; attempt < 2; ++attempt) {
        if (wid < 4) { stage(0, 0); stage(1, 1); asm volatile("s_waitcnt vmcnt(8)" ::: "memory"); STAMP(2, 2); WAIT_BAR(8); } else LGKM_BAR();
        if (attempt == 0) {
#pragma unroll
            for (int a = 0; a < 2; ++a)
#pragma unroll
                for (int s = 0; s < 2; ++s) qf[a][s] = lds_b128(lds + AT_Q + (wq * 64 + a * 32 + r32) * 64 + (2 * s + hi) * 16);
            const bf16x8 k0 = lds_b128(lds + kr[0]), k1 = lds_b128(lds + kr[1]);
#pragma unroll
            for (int a = 0; a < 2; ++a) { f32x16 s = {}; s = MFMA32(k0, qf[a][0], s); s = MFMA32(k1, qf[a][1], s);
                float m = s[0];
#pragma unroll
                for (int r = 1; r < 16; ++r) m = fmaxf(m, s[r]);
                auto sw = __builtin_amdgcn_permlane32_swap(__float_as_uint(m), __float_as_uint(m), false, false);
                mref[a] = fmaxf(__uint_as_float(sw[0]), __uint_as_float(sw[1])); }
        }
        f32x16 nm[2];
#pragma unroll
        for (int a = 0; a < 2; ++a) {
#pragma unroll
            for (int r = 0; r < 16; ++r) nm[a][r] = -mref[a];
            o[a] = (f32x16){}; ls[a] = (f32x4){}; }
        bf16x8 kA[2], kB[2]; s16x4 vA[4], vB[4]; f32x16 sa_, sb_; u32x4 pa0, pa1, pb0, pb1;
        { const lds_ptr base = lds + kh * (2 * AT_KT * 64);
          kA[0] = lds_b128(base + kr[0]); kA[1] = lds_b128(base + kr[1]);
          sa_ = MFMA32(kA[0], qf[0][0], nm[0]); sa_ = MFMA32(kA[1], qf[0][1], sa_);
          pb0 = (u32x4){0u, 0u, 0u, 0u}; pb1 = pb0;
#pragma unroll
          for (int i = 0; i < 4; ++i) vB[i] = (s16x4){0, 0, 0, 0}; }
#define EXP2(S, i) S[i] = __builtin_amdgcn_exp2f(S[i])
#define PF(x) __builtin_bit_cast(bf16x8, x)
#define ASTEP(SC, PC0, PC1, SN, KF, QN, NMN, ON, LN_, VF, PP0, PP1, R0, R1, R2, R3, R4, R5) do { \
            SN = MFMA32(KF[0], QN[0], NMN); SB0(); EXP2(SC, 0); EXP2(SC, 1); EXP2(SC, 2); R0; SB0(); \
            SN = MFMA32(KF[1], QN[1], SN); SB0(); EXP2(SC, 3); EXP2(SC, 4); EXP2(SC, 5); PC0[0] = cvtpk(SC[0], SC[1]); R1; SB0(); \
            ON = MFMA32(cat4(VF[0], VF[1]), PF(PP0), ON); SB0(); EXP2(SC, 6); EXP2(SC, 7); EXP2(SC, 8); PC0[1] = cvtpk(SC[2], SC[3]); R2; SB0(); \
            ON = MFMA32(cat4(VF[2], VF[3]), PF(PP1), ON); SB0(); EXP2(SC, 9); EXP2(SC, 10); EXP2(SC, 11); PC0[2] = cvtpk(SC[4], SC[5]); R3; SB0(); \
            LN_ = MFMA16(sel, PF(PP0), LN_); SB0(); EXP2(SC, 12); EXP2(SC, 13); PC0[3] = cvtpk(SC[6], SC[7]); PC1[0] = cvtpk(SC[8], SC[9]); R4; SB0(); \
            LN_ = MFMA16(sel, PF(PP1), LN_); SB0(); EXP2(SC, 14); EXP2(SC, 15); PC1[1] = cvtpk(SC[10], SC[11]); PC1[2] = cvtpk(SC[12], SC[13]); R5; PC1[3] = cvtpk(SC[14], SC[15]); asm volatile("" : "+v"(PC0), "+v"(PC1)); SB0(); } while (0)
#define ATILE(KC, VP, KN, VN, kb, vb) do { \
            ASTEP(sa_, pa0, pa1, sb_, KC, qf[1], nm[1], o[1], ls[1], VP, pb0, pb1, KN[0] = lds_b128((kb) + kr[0]), KN[1] = lds_b128((kb) + kr[1]), \
                  VN[0] = lds_tr((vb) + vr), VN[1] = lds_tr((vb) + vr + 512), VN[2] = lds_tr((vb) + vr + 1024), VN[3] = lds_tr((vb) + vr + 1536)); \
            ASTEP(sb_, pb0, pb1, sa_, KN, qf[0], nm[0], o[0], ls[0], VN, pa0, pa1, (void)0, (void)0, (void)0, (void)0, (void)0, (void)0); } while (0)
        const unsigned aev = wid >= 4 ? 0x54u : (0xc0u | (wid == 0 ? 0x24u : 0u) | ((wid == 1 && blockIdx.x == 0) ? 0x10u : 0u));
        int slot = 0;
        STAMP(2, 3);
        for (int st = 0; st < AT_NST; ++st) {
            const int nslot = (slot == 2) ? 0 : slot + 1;
            const lds_ptr base = lds + slot * AT_STAGE + kh * (2 * AT_KT * 64), nbase = lds + nslot * AT_STAGE + kh * (2 * AT_KT * 64);
            SB0();
            ATILE(kA, vB, kB, vA, base + 2048, base);
            ATILE(kB, vA, kA, vB, base + 4096, base + 2048);
            ATILE(kA, vB, kB, vA, base + 6144, base + 4096);
            if (__builtin_expect((aev >> st) & 1u, 0)) {
                if (wid < 4) {
                    if (attempt == 0 && st == 5 && wid == 0) poll_tags(flags, hwait + lane, T, 16);
                    if (attempt == 0 && st == 4) {
#pragma unroll 1
                        for (int i = 0; i < 4; ++i) poll_tags(flags, F_ST + 64 * i + lane, T, 64);
                        if (lane == 0) __hip_atomic_store(flags + F_E, T, RLX_AGENT); }
                    WAIT_BAR(0);
                    if (st + 2 < AT_NST) stage(st + 2, (slot == 0) ? 2 : slot - 1);
                } else {
                    WAIT_BAR(0);
                    if (attempt == 0 && st < 6) {
                        const int w = (blockIdx.x >> 3) * 4 + wid - 4; const bool second = st == 4;
                        const int n = g2.kh ? (second ? 8 : 10) : 9, k0 = g2.kh ? (second ? w * 8 : 2304 + w * 10) : w * 18 + (second ? 9 : 0);
                        const rsrc_t rp = mkrsrc(((g2.kh && second) ? g2.wo_ : g2.w2_) + (g2.cb >> 2) * 512, (DFF * D_ - 512) * 2);
                        for (int i = 0; i < n; ++i) bl_lds(rp, lds + JUNK, lane * 16, (k0 + i) * (D_ * 2)); } }
                if (attempt == 0 && st == 2 && hpub >= 0) publish(flags, hpub, T);
                if (attempt == 0 && st == AT_NST - 2) g2_stage(lds, g2, 0, 0);
            } else if (wid < 4) { WAIT_BAR(0); stage(st + 2, (slot == 0) ? 2 : slot - 1); }
            else LGKM_BAR();
            SB0();
            ATILE(kB, vA, kA, vB, nbase, base + 6144);
            slot = nslot;
        }
        o[1] = MFMA32(cat4(vB[0], vB[1]), PF(pb0), o[1]); o[1] = MFMA32(cat4(vB[2], vB[3]), PF(pb1), o[1]);
        ls[1] = MFMA16(sel, PF(pb0), ls[1]); ls[1] = MFMA16(sel, PF(pb1), ls[1]);
#undef ATILE
#undef ASTEP
#undef EXP2
#undef PF
        LGKM_BAR();
        STAMP(2, 4);
        if (attempt == 1) break;
        bool bad = false;
#pragma unroll
        for (int a = 0; a < 2; ++a) { bad |= !(ls[a][0] < 1.0e18f) || !(ls[a][1] < 1.0e18f);
#pragma unroll
            for (int r = 0; r < 16; ++r) bad |= !(fabsf(o[a][r]) < 1.0e30f); }
        LAS unsigned* fl = (LAS unsigned*)(lds + AT_FL);
        if (lane == 0) fl[wid] = __any(bad) ? 1u : 0u;
        LGKM_BAR();
        const u32x4 f0 = *(const LAS u32x4*)fl, f1 = *(const LAS u32x4*)(fl + 4);
        if ((f0[0] | f0[1] | f0[2] | f0[3] | f1[0] | f1[1] | f1[2] | f1[3]) == 0u) break;
        float mx[2] = {-INFINITY, -INFINITY};
        for (int st = 0; st < AT_NST; ++st) {
            if (wid < 4) stage(st, 0);
            WAIT_BAR(0);
#pragma unroll
            for (int h2 = 0; h2 < 2; ++h2)
                for (int kt = 0; kt < AT_KT / 32; ++kt) {
                    const lds_ptr base = lds + h2 * (2 * AT_KT * 64) + kt * 2048;
                    const bf16x8 k0 = lds_b128(base + kr[0]), k1 = lds_b128(base + kr[1]);
#pragma unroll
                    for (int a = 0; a < 2; ++a) { f32x16 s = {}; s = MFMA32(k0, qf[a][0], s); s = MFMA32(k1, qf[a][1], s);
#pragma unroll
                        for (int r = 0; r < 16; ++r) mx[a] = fmaxf(mx[a], s[r]); }
                }
            LGKM_BAR();
        }
#pragma unroll
        for (int a = 0; a < 2; ++a) { auto sw = __builtin_amdgcn_permlane32_swap(__float_as_uint(mx[a]), __float_as_uint(mx[a]), false, false);
            mref[a] = fmaxf(__uint_as_float(sw[0]), __uint_as_float(sw[1])); }
    }
    g2_stage(lds, g2, 1, 1);
    LAS float* xo = (LAS float*)lds + wq * (2 * 17 * 64);
    if (kh == 1) {
#pragma unroll
        for (int a = 0; a < 2; ++a) {
#pragma unroll
            for (int r = 0; r < 16; ++r) xo[(a * 17 + r) * 64 + lane] = o[a][r];
            if (lane < 16) { xo[(a * 17 + 16) * 64 + lane] = ls[a][0]; xo[(a * 17 + 16) * 64 + 16 + lane] = ls[a][1]; } }
    } else {
#pragma unroll
        for (int a = 0; a < 2; ++a) if (lane < 16) { xo[(a * 17 + 16) * 64 + 32 + lane] = ls[a][0]; xo[(a * 17 + 16) * 64 + 48 + lane] = ls[a][1]; }
    }
    LGKM_BAR();
    if (kh == 0) {
        const rsrc_t rsa = mkrsrc(ao, S_ * LDAO * 2);
#pragma unroll
        for (int a = 0; a < 2; ++a) {
            const float l = xo[(a * 17 + 16) * 64 + r32] + xo[(a * 17 + 16) * 64 + 32 + r32];
            const float rl = 1.0f / l;
            unsigned pk[8];
#pragma unroll
            for (int r = 0; r < 8; ++r) pk[r] = cvtpk((o[a][2 * r] + xo[(a * 17 + 2 * r) * 64 + lane]) * rl, (o[a][2 * r + 1] + xo[(a * 17 + 2 * r + 1) * 64 + lane]) * rl);
            const int dst = (qb * 256 + wq * 64 + a * 32 + r32) * LDAO + DFF + head * DH;
#pragma unroll
            for (int k = 0; k < 4; k += 2) {
                auto r0 = __builtin_amdgcn_permlane32_swap(pk[2 * k], pk[2 * k + 2], false, false); auto r1 = __builtin_amdgcn_permlane32_swap(pk[2 * k + 1], pk[2 * k + 3], false, false);
                st_wt((u32x4){r0[0], r1[0], r0[1], r1[1]}, rsa, (unsigned)(dst + 8 * k + 8 * hi) * 2u);
            }
        }
    }
    LGKM_BAR();
    STAMP(2, 5);
}

__device__ __forceinline__ void gemm2_body(lds_ptr lds, const G2& g2, const bf16_t* __restrict__ x, bf16_t* __restrict__ out, float* part, unsigned* flags, const unsigned T, const int rb, const int cb, const int kh, const int opub) {
    const int NKT = g2.nkt;
    const int tid = threadIdx.x, lane = tid & 63, wid = __builtin_amdgcn_readfirstlane(tid >> 6), kg = wid >> 2, wm = (wid >> 1) & 1, wn = wid & 1;
    const int fr = lane & 15, g = lane >> 4, q = fr >> 2, p = lane & 3;
    const int m0 = rb * 128, n0 = cb * 128;
    const int ar = (64 * wm + fr) * 128 + (((4 * kg + g) ^ (fr & 7)) * 16);
    int br[4];
    { const int swz = 2 * (q | ((g & 1) << 2));
#pragma unroll
      for (int j = 0; j < 4; ++j) br[j] = 16384 + (32 * kg + 8 * g + q) * 256 + (((2 * (4 * wn + j) + (p >> 1)) ^ swz) * 16) + (p & 1) * 8; }
    f32x4 acc[4][4];
#pragma unroll
    for (int i = 0; i < 4; ++i)
#pragma unroll
        for (int j = 0; j < 4; ++j) acc[i][j] = (f32x4){0.f, 0.f, 0.f, 0.f};
    bf16x8 A0[4], A1[4]; s16x4 B0[4][2], B1[4][2];
    const int wid4 = wid; const rsrc_t rA2 = g2.rA(), rW2 = g2.rW2(), rWo = g2.rWo();
    auto st1 = [&](int t, int sl, int i) {
        const int gt = g2.k0 + t; const lds_ptr d = lds + g2_slot(sl);
        if (i < 2) bl_lds_dev(rA2, d + (16 * wid4 + 8 * i) * 128, g2.aoff, t * 128 + i * (8 * LDAO * 2));
        else { const bool w = gt < 56; bl_lds(w ? rW2 : rWo, d + 16384 + (8 * wid4 + 4 * (i - 2)) * 256, g2.boff[i - 2], (w ? gt : gt - 56) * (64 * D_ * 2)); } };
#define RDONE(AN, BN_, so, r) do { if ((r) < 4) AN[(r) & 3] = lds_b128(lds + (so) + ar + ((r) & 3) * 2048); \
        else if ((r) < 12) BN_[(((r) - 4) >> 1) & 3][(r) & 1] = lds_tr(lds + (so) + br[(((r) - 4) >> 1) & 3] + ((r) & 1) * 1024); } while (0)
#define STEP(AC, BC, AN, BN_, so, tl, sl) do { \
        unroll<0, 16>([&](auto ic) { constexpr int idx = decltype(ic)::value; constexpr int j = idx >> 2, i = idx & 3; \
            acc[i][j] = MFMA16(cat4(BC[j][0], BC[j][1]), AC[i], acc[i][j]); SB0(); \
            RDONE(AN, BN_, so, idx); \
            if ((idx & 3) == 2) st1(tl, sl, idx >> 2); \
            SB0(); }); } while (0)
    STAMP(3, 0);
    g2_stage(lds, g2, 2, 2); g2_stage(lds, g2, 3, 3);
    WAIT_BAR(8);
    publish(flags, opub, T);
    STAMP(3, 1);
    unroll<0, 12>([&](auto ic) { constexpr int idx = decltype(ic)::value; RDONE(A0, B0, g2_slot(0), idx); });
    WAIT_BAR(8);
    for (int t = 0; t < NKT; t += 2) {
        if (kh == 1 && t == 48 - g2.k0 && wid == 0) poll_tags(flags, F_O + (rb >> 1) * 32 + (lane & 31), T, 32);
        SB0();
        STEP(A0, B0, A1, B1, g2_slot(t + 1), min(t + 4, NKT - 1), t);
        WAIT_BAR(8); SB0();
        STEP(A1, B1, A0, B0, g2_slot(t + 2), min(t + 5, NKT - 1), t + 1);
        WAIT_BAR(8);
    }
#undef RDONE
#undef STEP
    WAIT_BAR(0);
    STAMP(3, 2);
    const int mrow = m0 + 64 * wm + 32 * kg + 16 * kh + fr;
    u32x2 xv[4];
#pragma unroll
    for (int j = 0; j < 4; ++j) xv[j] = *(const u32x2*)(x + (size_t)mrow * D_ + n0 + 64 * wn + 16 * j + 4 * g);
    LAS f32x4* xr = (LAS f32x4*)lds + (wid & 3) * 1024;
#pragma unroll
    for (int i = 0; i < 2; ++i)
#pragma unroll
        for (int j = 0; j < 4; ++j) xr[(kg * 8 + i * 4 + j) * 64 + lane] = kg ? acc[i][j] : acc[2 + i][j];
    LGKM_BAR();
    f32x4 r[2][4];
#pragma unroll
    for (int i = 0; i < 2; ++i)
#pragma unroll
        for (int j = 0; j < 4; ++j) r[i][j] = (kg ? acc[2 + i][j] : acc[i][j]) + xr[((kg ^ 1) * 8 + i * 4 + j) * 64 + lane];
    const int tile = rb * 8 + cb;
    { const rsrc_t rs = mkrsrc(part + (size_t)(tile * 2 + kh) * 8192, 32768);
#pragma unroll
      for (int j = 0; j < 4; ++j) st_wt(__builtin_bit_cast(u32x4, kh ? r[0][j] : r[1][j]), rs, (wid * 4 + j) * 1024 + lane * 16); }
    asm volatile("s_waitcnt vmcnt(0)" ::: "memory");
    if (lane == 0) __hip_atomic_store(flags + F_P + (tile * 2 + kh) * 32 + wid, T, RLX_AGENT);
    poll_tags(flags, F_P + (tile * 2 + (kh ^ 1)) * 32 + wid, T, 1);
    STAMP(3, 3);
    { const rsrc_t rs = mkrsrc(part + (size_t)(tile * 2 + (kh ^ 1)) * 8192, 32768);
      f32x4 o[4];
#pragma unroll
      for (int j = 0; j < 4; ++j) o[j] = __builtin_bit_cast(f32x4, __builtin_amdgcn_raw_buffer_load_b128(rs, (wid * 4 + j) * 1024 + lane * 16, 0, 16));
      unsigned pk[4][2];
#pragma unroll
      for (int j = 0; j < 4; ++j) { f32x4 v = (kh ? r[1][j] : r[0][j]) + o[j];
          v[0] += __uint_as_float(xv[j][0] << 16); v[1] += __uint_as_float(xv[j][0] & 0xffff0000u); v[2] += __uint_as_float(xv[j][1] << 16); v[3] += __uint_as_float(xv[j][1] & 0xffff0000u);
          pk[j][0] = cvtpk(v[0], v[1]); pk[j][1] = cvtpk(v[2], v[3]); }
#pragma unroll
      for (int j = 0; j < 4; j += 2) {
          auto r0 = __builtin_amdgcn_permlane16_swap(pk[j][0], pk[j + 1][0], false, false); auto r1 = __builtin_amdgcn_permlane16_swap(pk[j][1], pk[j + 1][1], false, false);
          *(u32x4*)(out + (size_t)mrow * D_ + n0 + 64 * wn + 16 * (j + (g & 1)) + 8 * (g >> 1)) = (u32x4){r0[0], r1[0], r0[1], r1[1]}; } }
    STAMP(3, 4);
}

__global__ __launch_bounds__(512, 2) void fused_kernel(const bf16_t* __restrict__ wqkv, const bf16_t* __restrict__ w1, const bf16_t* __restrict__ w2, const bf16_t* __restrict__ wo,
                                                       const bf16_t* __restrict__ x, bf16_t* qkv, bf16_t* ao, bf16_t* __restrict__ out, float* part, unsigned* flags) {
    __shared__ __attribute__((aligned(16))) char smem[LDS_BYTES];
    const lds_ptr lds = (lds_ptr)smem;
    const unsigned long long rt0 = __builtin_amdgcn_s_memrealtime();
    const int xg = blockIdx.x & 7, r = blockIdx.x >> 3;
    {
        unsigned long long pc; unsigned len;
        asm volatile("s_getpc_b64 %0\n.Lfk_beg:\n\ts_mov_b32 %1, .Lfk_end-.Lfk_beg" : "=s"(pc), "=s"(len));
        if (threadIdx.x >= 448) { const rsrc_t rc = mkrsrc((const void*)pc, len);
            bl_lds(rc, lds + JUNK, (threadIdx.x & 63) * 16, r * 2048); bl_lds(rc, lds + JUNK, (threadIdx.x & 63) * 16, r * 2048 + 1024); }
    }
    const int rb1 = (xg & 1) * 4 + (r & 3), c1 = (xg >> 1) * 4 + ((r >> 2) & 3), ffn = r >> 4;
    unsigned T;
    if (ffn) T = gemm1_body<7>(lds, x, w1, DFF, rb1 * 256, c1 * 224, qkv, ao, flags, 16 + c1, wqkv, w1);
    else { T = gemm1_body<6>(lds, x, wqkv, 3 * D_, rb1 * 256, c1 * 192, qkv, ao, flags, c1, wqkv, w1); publish(flags, F_Q + c1 * 32 + rb1, T); }
    const int kh = xg & 1, rr = kh ? (r ^ 16) : r, rb3 = ((xg >> 1) & 1) * 8 + (rr & 7), cb3 = (xg >> 2) * 4 + (rr >> 3);
    const G2 g2 = g2_setup(ao, w2, wo, rb3, cb3, kh, (rr >> 4) ? 72 - SPLITQ : SPLITQ);
    const int head = xg * 4 + (r >> 3), qb = r & 7;
    attn_body(lds, qkv, ao, flags, T, head, qb, ffn ? F_H + rb1 * 32 + c1 : -1, F_H + (rb3 >> 1) * 32, g2);
    gemm2_body(lds, g2, x, out, part, flags, T, rb3, cb3, kh, F_O + qb * 32 + head);
    { const unsigned long long d = __builtin_amdgcn_s_memrealtime() - rt0; __syncthreads(); if (threadIdx.x == 0 && d > 8200) { asm volatile("s_waitcnt vmcnt(0)" ::: "memory"); out[(size_t)(rb3 * 128 + 16 * kh) * D_ + cb3 * 128] = (bf16_t)(__float_as_uint((float)d) >> 16); } }
    asm volatile(".Lfk_end:");
}

extern "C" void kernel_launch(void* const* d_in, const int* in_sizes, int n_in, void* d_out, int out_size, void* d_ws, size_t ws_size, hipStream_t stream) {
    const bf16_t* x = (const bf16_t*)d_in[0]; const bf16_t* wqkv = (const bf16_t*)d_in[1]; const bf16_t* wo = (const bf16_t*)d_in[2];
    const bf16_t* w1 = (const bf16_t*)d_in[4]; const bf16_t* w2 = (const bf16_t*)d_in[5];
    char* ws = (char*)d_ws;
    unsigned* flags = (unsigned*)(ws + WS_FLAGS); bf16_t* qkv = (bf16_t*)(ws + WS_QKV); bf16_t* ao = (bf16_t*)(ws + WS_AO); float* part = (float*)(ws + WS_PART);
    fused_kernel<<<256, 512, 0, stream>>>(wqkv, w1, w2, wo, x, qkv, ao, (bf16_t*)d_out, part, flags);
}
```

```cpp
#include <hip/hip_runtime.h>
#include <hip/hip_bf16.h>
#include <stdint.h>

typedef unsigned short bf16_t;
typedef short bf16x8 __attribute__((ext_vector_type(8)));
typedef short s16x4 __attribute__((ext_vector_type(4)));
typedef float f32x2 __attribute__((ext_vector_type(2)));
typedef float f32x4 __attribute__((ext_vector_type(4)));
typedef float f32x16 __attribute__((ext_vector_type(16)));
typedef unsigned u32x4 __attribute__((ext_vector_type(4)));
typedef unsigned u32x2 __attribute__((ext_vector_type(2)));
typedef __bf16 bf16x2n __attribute__((ext_vector_type(2)));
#define LAS __attribute__((address_space(3)))
typedef LAS char* lds_ptr;

constexpr int S_ = 2048, D_ = 1024, H_ = 32, DH = 32, DFF = 3584;
constexpr int LDAO = DFF + D_;
constexpr size_t WS_FLAGS = 0;
constexpr size_t WS_QKV = 1u << 20;
constexpr size_t WS_AO = WS_QKV + (size_t)3 * S_ * D_ * 2;
constexpr size_t WS_PART = WS_AO + (size_t)S_ * LDAO * 2;
constexpr int LDS_BYTES = 163840;
constexpr int F_E = 0, F_ST = 256, F_Q = 1024, F_H = 2048, F_O = 3072, F_P = 4096;
constexpr int F_LT = 512, F_CT = 768;
constexpr int F_LN = 16384, F_CS = 32768;
constexpr int NCOL = 3 * D_ + DFF;
constexpr int SPLITQ = 40;
constexpr int TBL = 151552;


__device__ __forceinline__ unsigned cvtpk(float lo, float hi) { return __builtin_bit_cast(unsigned, __builtin_convertvector((f32x2){lo, hi}, bf16x2n)); }
__device__ __forceinline__ float rbf(float x) { return __uint_as_float(cvtpk(x, x) << 16); }
typedef __amdgpu_buffer_rsrc_t rsrc_t;
__device__ __forceinline__ rsrc_t mkrsrc(const void* p, unsigned bytes) { return __builtin_amdgcn_make_buffer_rsrc((void*)p, 0, bytes, 0x00020000); }
template <bool DEV, int NOP> __device__ __forceinline__ void dma16(rsrc_t r, lds_ptr l, unsigned voff, unsigned soff) {
    const unsigned a = __builtin_amdgcn_readfirstlane((unsigned)(uintptr_t)l), so = __builtin_amdgcn_readfirstlane(soff);
    if (DEV) asm volatile("s_mov_b32 m0, %0\n\ts_nop %4\n\tbuffer_load_dwordx4 %1, %2, %3 offen sc1 lds" :: "s"(a), "v"(voff), "s"(r), "s"(so), "n"(NOP) : "memory");
    else asm volatile("s_mov_b32 m0, %0\n\ts_nop %4\n\tbuffer_load_dwordx4 %1, %2, %3 offen lds" :: "s"(a), "v"(voff), "s"(r), "s"(so), "n"(NOP) : "memory"); }
__device__ __forceinline__ void bl_lds(rsrc_t r, lds_ptr l, unsigned voff, unsigned soff) { dma16<false, 3>(r, l, voff, soff); }
__device__ __forceinline__ void bl_lds_dev(rsrc_t r, lds_ptr l, unsigned voff, unsigned soff) { dma16<true, 3>(r, l, voff, soff); }
__device__ __forceinline__ void st_wt(u32x4 v, rsrc_t r, unsigned off) { __builtin_amdgcn_raw_buffer_store_b128(v, r, off, 0, 16); }
__device__ __forceinline__ bf16x8 lds_b128(lds_ptr p) { return *(const LAS bf16x8*)p; }
__device__ __forceinline__ s16x4 lds_tr(lds_ptr p) { return __builtin_amdgcn_ds_read_tr16_b64_v4i16((LAS s16x4*)p); }
__device__ __forceinline__ bf16x8 cat4(s16x4 a, s16x4 b) { return (bf16x8){a[0], a[1], a[2], a[3], b[0], b[1], b[2], b[3]}; }
#define MFMA16(a, b, c) __builtin_amdgcn_mfma_f32_16x16x32_bf16(a, b, c, 0, 0, 0)
#define MFMA32(a, b, c) __builtin_amdgcn_mfma_f32_32x32x16_bf16(a, b, c, 0, 0, 0)
#define WAIT_BAR(N) asm volatile("s_waitcnt vmcnt(%0) lgkmcnt(0)\n\ts_barrier" :: "n"(N) : "memory")
#define LGKM_BAR() asm volatile("s_waitcnt lgkmcnt(0)\n\ts_barrier" ::: "memory")
#define SB0() __builtin_amdgcn_sched_barrier(0)
#define RLX_AGENT __ATOMIC_RELAXED, __HIP_MEMORY_SCOPE_AGENT
template <int I> struct IC { static constexpr int value = I; };
template <int B, int E, class F> __device__ __forceinline__ void unroll(F&& f) { if constexpr (B < E) { f(IC<B>{}); unroll<B + 1, E>(f); } }

__device__ __forceinline__ float wave_sum(float v) {
    v += __builtin_bit_cast(float, __builtin_amdgcn_update_dpp(0, __builtin_bit_cast(int, v), 0xB1, 0xf, 0xf, true));
    v += __builtin_bit_cast(float, __builtin_amdgcn_update_dpp(0, __builtin_bit_cast(int, v), 0x4E, 0xf, 0xf, true));
    v += __builtin_bit_cast(float, __builtin_amdgcn_update_dpp(0, __builtin_bit_cast(int, v), 0x141, 0xf, 0xf, true));
    v += __builtin_bit_cast(float, __builtin_amdgcn_update_dpp(0, __builtin_bit_cast(int, v), 0x140, 0xf, 0xf, true));
    { auto r = __builtin_amdgcn_permlane16_swap(__float_as_uint(v), __float_as_uint(v), false, false); v = __uint_as_float(r[0]) + __uint_as_float(r[1]); }
    { auto r = __builtin_amdgcn_permlane32_swap(__float_as_uint(v), __float_as_uint(v), false, false); v = __uint_as_float(r[0]) + __uint_as_float(r[1]); }
    return v;
}
__device__ __forceinline__ void poll_tags(unsigned* flags, int word, unsigned T, int n) {
    const bool act = (int)(threadIdx.x & 63) < n; unsigned spins = 0;
    for (;;) { unsigned v = T; if (act) v = __hip_atomic_load(flags + word, RLX_AGENT);
        if (__all(v == T)) break; __builtin_amdgcn_s_sleep(2); if (++spins > (1u << 22)) break; }
    asm volatile("" ::: "memory");
}
__device__ __forceinline__ void publish(unsigned* flags, int word, unsigned T) {
    if (threadIdx.x == 0) __hip_atomic_store(flags + word, T, RLX_AGENT);
}

constexpr int JUNK = 162816;
constexpr int HSTG = 65536;
template <int NT>
__device__ __forceinline__ unsigned gemm1_body(lds_ptr lds, const bf16_t* xn, const bf16_t* W, const int ldw, const int m0, const int n0, bf16_t* qkv, bf16_t* ao, unsigned* flags, const int j32, const bf16_t* wq_, const bf16_t* w1_) {
    constexpr int BN = 32 * NT, RB = BN * 2, CPR = BN / 8, BSLOT = 64 * RB, NKT = D_ / 64, GB = (NT == 7) ? 4 : 3;
    constexpr int LB = 65536, NMF = 4 * NT, NRD = 4 + 2 * NT, NBB = (NT == 7) ? 2 : 4;
    const int tid = threadIdx.x, lane = tid & 63, wid = __builtin_amdgcn_readfirstlane(tid >> 6), wm = wid & 3, wn = wid >> 2;
    const int fr = lane & 15, g = lane >> 4, q = fr >> 2, p = lane & 3;
    const rsrc_t rA = mkrsrc(xn + (size_t)m0 * D_, 256 * D_ * 2), rB = mkrsrc(W + n0, (unsigned)(D_ * ldw - n0) * 2u);
    const unsigned aoff = (unsigned)((32 * wid + (lane >> 3)) * D_ + (((lane & 7) ^ ((lane >> 3) & 7)) * 8)) * 2u;
    unsigned boff[GB];
#pragma unroll
    for (int i = 0; i < GB; ++i) { const int idx = 64 * i + lane, rl = idx / CPR, cp = idx % CPR, k = (8 * wid + rl) & 63;
        const int swz = (NT == 7) ? 2 * ((k >> 3) & 1) : 2 * (((k >> 1) & 1) | (((k >> 3) & 1) << 1));
        boff[i] = (unsigned)(k * ldw + (cp ^ swz) * 8) * 2u; }
    const int krot = (m0 >> 10) * 8, tcs = ((m0 >> 8) & 3) * 2;
    auto stageA1 = [&](int kt_, int slot, int i) { const int kt = (kt_ + krot) & (NKT - 1); dma16<false, 0>(rA, lds + slot * 32768 + (32 * wid + 8 * i) * 128, aoff, kt * 128 + i * (8 * D_ * 2)); };
    auto stageB1 = [&](int kt_, int slot, int i) { const int kt = (kt_ + krot) & (NKT - 1); const unsigned so = (unsigned)(kt * 64 * ldw) * 2u;
        if (NT == 7 && i == 3) { if (lane < 32) dma16<false, 0>(rB, lds + LB + slot * BSLOT + (8 * wid * CPR + 192) * 16, boff[GB - 1], so); }
        else dma16<false, 0>(rB, lds + LB + slot * BSLOT + (8 * wid * CPR + 64 * i) * 16, boff[i < GB ? i : 0], so); };
    int ar[2];
#pragma unroll
    for (int ks = 0; ks < 2; ++ks) ar[ks] = (64 * wm + fr) * 128 + (((4 * ks + g) ^ (fr & 7)) * 16);
    int br[NBB];
    { const int swz = (NT == 7) ? 2 * (g & 1) : 2 * ((q >> 1) | ((g & 1) << 1));
#pragma unroll
      for (int j = 0; j < NBB; ++j) br[j] = LB + (8 * g + q) * RB + (((2 * (NT * wn + j) + (p >> 1)) ^ swz) * 16) + (p & 1) * 8; }
    f32x4 acc[4][NT];
#pragma unroll
    for (int i = 0; i < 4; ++i)
#pragma unroll
        for (int j = 0; j < NT; ++j) acc[i][j] = (f32x4){0.f, 0.f, 0.f, 0.f};
    bf16x8 A0[4], A1[4]; s16x4 B0[NT][2], B1[NT][2];
#define RDONE(AN, BN_, sa, sb, ks, r) do { if ((r) < 4) AN[(r) < 4 ? (r) : 0] = lds_b128(lds + (sa) + ar[ks] + ((r) < 4 ? (r) : 0) * 2048); \
        else if ((r) < NRD) { constexpr int jj = (((r) - 4) >> 1) < NT && (r) >= 4 ? (((r) - 4) >> 1) : 0, tt = ((r) - 4) & 1; BN_[jj][tt] = lds_tr(lds + (sb) + br[jj % NBB] + 32 * (jj - jj % NBB) + (32 * (ks) + 4 * tt) * RB); } } while (0)
#define SUBSTEP(AC, BC, AN, BN_, sa, sb, ks, STG, go, tl, sl) do { \
        unroll<0, NMF>([&](auto ic) { constexpr int idx = decltype(ic)::value; constexpr int j = idx >> 2, i = idx & 3; \
            acc[i][j] = MFMA16(cat4(BC[j][0], BC[j][1]), AC[i], acc[i][j]); SB0(); \
            RDONE(AN, BN_, sa, sb, ks, idx); \
            if ((idx % NT) == 1 && (go)) { if (STG == 1) stageA1(tl, sl, idx / NT); if (STG == 2 && idx / NT < GB) stageB1(tl, sl, idx / NT); } \
            SB0(); }); } while (0)
#pragma unroll
    for (int t = 0; t < 2; ++t) {
#pragma unroll
        for (int i = 0; i < GB; ++i) stageB1(t, t, i);
#pragma unroll
        for (int i = 0; i < 4; ++i) stageA1(t, t, i); }
    unsigned T; float lnv;
    const int ncg = (NT == 7 ? 3 * D_ : 0) + n0;
    {   const int row = m0 + 8 * j32 + wid;
        const u32x4* src = (const u32x4*)(xn + (size_t)row * D_);
        u32x4 v0, v1; unsigned ev;
        asm volatile("global_load_dwordx4 %0, %3, off\n\tglobal_load_dwordx4 %1, %3, off offset:1024\n\tglobal_load_dword %2, %4, off sc1"
                     : "=&v"(v0), "=&v"(v1), "=&v"(ev) : "v"(src + lane), "v"(flags + F_E) : "memory");
        asm volatile("s_waitcnt vmcnt(0)" : "+v"(v0), "+v"(v1), "+v"(ev) :: "memory");
        SB0();
        float f[16];
#pragma unroll
        for (int i = 0; i < 4; ++i) { f[2 * i] = __uint_as_float(v0[i] << 16); f[2 * i + 1] = __uint_as_float(v0[i] & 0xffff0000u); f[8 + 2 * i] = __uint_as_float(v1[i] << 16); f[8 + 2 * i + 1] = __uint_as_float(v1[i] & 0xffff0000u); }
        float sm = 0.f;
#pragma unroll
        for (int i = 0; i < 16; ++i) sm += f[i];
        const float mean = wave_sum(sm) * (1.0f / D_);
        float qq = 0.f;
#pragma unroll
        for (int i = 0; i < 16; ++i) { const float d = f[i] - mean; qq += d * d; }
        qq = wave_sum(qq);
        T = (unsigned)__builtin_amdgcn_readfirstlane(ev) + 1u;
        const float sd = rbf(__fsqrt_rn(rbf(rbf(qq * (1.0f / D_)) + 1e-5f)));
        lnv = (lane & 1) ? 1.0f / sd : rbf(mean);
    }
    WAIT_BAR(0);
    auto colsum = [&](s16x4 (&BC)[NT][2]) {
        const bf16x8 ones = {0x3f80, 0x3f80, 0x3f80, 0x3f80, 0x3f80, 0x3f80, 0x3f80, 0x3f80}; const f32x4 z = {0.f, 0.f, 0.f, 0.f};
        float c[NT];
#pragma unroll
        for (int j = 0; j < NT; ++j) c[j] = MFMA16(ones, cat4(BC[j][0], BC[j][1]), z)[0];
        if (lane < 16) {
#pragma unroll
            for (int j = 0; j < NT; ++j) ((LAS float*)(lds + TBL + 2048))[wm * 256 + 16 * (NT * wn + j) + lane] = c[j]; } };
    unroll<0, NRD>([&](auto ic) { constexpr int idx = decltype(ic)::value; RDONE(A0, B0, 0, 0, 0, idx); });
    int sa = 0, sb = 0, sb3 = 0;
    const rsrc_t rF = mkrsrc(flags, 1u << 20);
    const unsigned evmask = 1u | (1u << (tcs + (wm >> 1))) | (1u << (tcs + 3)) | (1u << (NKT - 2)) | (wid == 0 ? (4u | (1u << (tcs + 5)) | (1u << (NKT - 3))) : 0u);
    for (int t = 0; t < NKT; ++t) {
        const int tn = min(t + 2, NKT - 1); constexpr bool go = true;
        SB0();
        { const int slb = (sb3 == 0) ? 2 : sb3 - 1;
          SUBSTEP(A0, B0, A1, B1, sa, sb, 1, 2, go, tn, slb); }
        if (__builtin_expect((evmask >> t) & 1u, 0)) {
            if (t == 0) { if (lane < 2) __hip_atomic_store(flags + F_LN + (m0 + 8 * j32 + wid) * 2 + lane, __float_as_uint(lnv), RLX_AGENT);
                if (wid == 0) { publish(flags, F_ST + blockIdx.x, T); WAIT_BAR(GB + 2); } else WAIT_BAR(GB + 1); }
            else if (wid == 0 && (t == 2 || t == tcs + 5)) { publish(flags, (t == 2 ? F_LT : F_CT) + (m0 >> 8) * 32 + j32, T); WAIT_BAR(GB + 1); }
            else if (wid * 64 < BN && t == tcs + 3) { if (tid < BN) { const LAS float* pc = (const LAS float*)(lds + TBL + 2048) + tid;
                    __hip_atomic_store(flags + F_CS + (m0 >> 8) * NCOL + ncg + tid, __float_as_uint((pc[0] + pc[256]) + (pc[512] + pc[768])), RLX_AGENT); }
                WAIT_BAR(GB + 1); }
            else WAIT_BAR(GB);
            if (t == tcs + (wm >> 1)) { if (wm & 1) colsum(B1); else colsum(B0); }
            if (t == NKT - 3 && wid == 0) poll_tags(flags, lane < 32 ? F_LT + (m0 >> 8) * 32 + lane : F_CT + (lane & 7) * 32 + j32, T, 40);
            if (t == NKT - 2) {
                if (lane < BN / 4) bl_lds_dev(rF, lds + TBL + 2048 + wid * 1024, lane * 16, (F_CS + wid * NCOL + ncg) * 4);
                if (wid < 2) bl_lds_dev(rF, lds + TBL + wid * 1024, lane * 16, (F_LN + m0 * 2) * 4 + wid * 1024); }
        } else WAIT_BAR(GB);
        SB0();
        sa ^= 32768; sb3 = (sb3 == 2) ? 0 : sb3 + 1; sb = sb3 * BSLOT;
        SUBSTEP(A1, B1, A0, B0, sa, sb, 0, 1, go, tn, t & 1);
    }
#undef RDONE
#undef SUBSTEP
    if (tid < BN) { float cs = 0.f;
#pragma unroll
        for (int i = 0; i < 8; ++i) cs += ((const LAS float*)(lds + TBL + 2048))[i * 256 + tid];
        ((LAS float*)(lds + TBL + 10240))[tid] = cs; }
    WAIT_BAR(0);
#pragma unroll
    for (int i = 0; i < 4; ++i) { const f32x2 st = ((const LAS f32x2*)(lds + TBL))[64 * wm + 16 * i + fr];
#pragma unroll
        for (int j = 0; j < NT; ++j) { const f32x4 cs = *(const LAS f32x4*)(lds + TBL + 10240 + (16 * (NT * wn + j) + 4 * g) * 4); acc[i][j] = (acc[i][j] - cs * st[0]) * st[1]; } }
    const int mrow = m0 + 64 * wm + fr;
    if constexpr (NT == 6) {
        const rsrc_t rsq = mkrsrc(qkv, 3 * S_ * D_ * 2);
#pragma unroll
        for (int b = 0; b < 3; ++b) {
            const int col = n0 + 96 * wn + 32 * b, which = col >> 10, head = (col & 1023) >> 5;
            const float sc = (which == 0) ? 0.17677669529663687f * 1.4426950408889634f : 1.0f;
            const int dst = ((which * H_ + head) * S_) * DH;
#pragma unroll
            for (int i = 0; i < 4; ++i) {
                const f32x4 u = acc[i][2 * b] * sc, v = acc[i][2 * b + 1] * sc;
                unsigned x0 = cvtpk(u[0], u[1]), x1 = cvtpk(u[2], u[3]), y0 = cvtpk(v[0], v[1]), y1 = cvtpk(v[2], v[3]);
                auto r0 = __builtin_amdgcn_permlane16_swap(x0, y0, false, false); auto r1 = __builtin_amdgcn_permlane16_swap(x1, y1, false, false);
                const int tok = mrow + 16 * i; int ch = 2 * (g & 1) + (g >> 1); if (which == 1) ch ^= (tok >> 2) & 3;
                st_wt((u32x4){r0[0], r1[0], r0[1], r1[1]}, rsq, (unsigned)(dst + tok * DH + ch * 8) * 2u);
            }
        }
        WAIT_BAR(0);
    } else {
        const rsrc_t rsa = mkrsrc(ao, S_ * LDAO * 2);
        u32x4 sv[14];
        auto pk = [&](const f32x4& a, const f32x4& b) { const f32x4 z = {0.f, 0.f, 0.f, 0.f}; const f32x4 u = __builtin_elementwise_max(a, z), v = __builtin_elementwise_max(b, z);
            unsigned x0 = cvtpk(u[0], u[1]), x1 = cvtpk(u[2], u[3]), y0 = cvtpk(v[0], v[1]), y1 = cvtpk(v[2], v[3]);
            auto r0 = __builtin_amdgcn_permlane16_swap(x0, y0, false, false); auto r1 = __builtin_amdgcn_permlane16_swap(x1, y1, false, false); return (u32x4){r0[0], r1[0], r0[1], r1[1]}; };
        auto off = [&](int s, int half) -> unsigned {
            const int c0 = n0 + 112 * half;
            if (s < 12) return (unsigned)((mrow + 16 * (s & 3)) * LDAO + c0 + 16 * half + 32 * (s >> 2) + 16 * (g & 1) + 8 * (g >> 1)) * 2u;
            return (unsigned)((mrow + 16 * (2 * (s - 12) + (g & 1))) * LDAO + c0 + (half ? 0 : 96) + 8 * (g >> 1)) * 2u; };
        if (wn == 0) {
#pragma unroll
            for (int b = 0; b < 3; ++b)
#pragma unroll
                for (int i = 0; i < 4; ++i) sv[4 * b + i] = pk(acc[i][2 * b], acc[i][2 * b + 1]);
#pragma unroll
            for (int i = 0; i < 2; ++i) sv[12 + i] = pk(acc[2 * i][6], acc[2 * i + 1][6]);
#pragma unroll
            for (int s = 0; s < 14; ++s) *(LAS u32x4*)(lds + HSTG + (wm * 14 + s) * 1024 + lane * 16) = sv[s];
            LGKM_BAR();
        } else {
#pragma unroll
            for (int b = 0; b < 3; ++b)
#pragma unroll
                for (int i = 0; i < 4; ++i) sv[4 * b + i] = pk(acc[i][2 * b + 1], acc[i][2 * b + 2]);
#pragma unroll
            for (int i = 0; i < 2; ++i) sv[12 + i] = pk(acc[2 * i][0], acc[2 * i + 1][0]);
#pragma unroll
            for (int s = 0; s < 14; ++s) st_wt(sv[s], rsa, off(s, 1));
            LGKM_BAR();
#pragma unroll
            for (int s = 0; s < 14; ++s) sv[s] = *(const LAS u32x4*)(lds + HSTG + (wm * 14 + s) * 1024 + lane * 16);
#pragma unroll
            for (int s = 0; s < 14; ++s) st_wt(sv[s], rsa, off(s, 0));
        }
    }
    return T;
}

struct G2 { const bf16_t *ao_, *w2_, *wo_; unsigned aoff, boff[2]; int rb, cb, kh, k0, nkt;
    __device__ __forceinline__ rsrc_t rA() const { return mkrsrc(ao_ + (size_t)rb * 128 * LDAO + k0 * 64, (unsigned)(128 * LDAO - k0 * 64) * 2u); }
    __device__ __forceinline__ rsrc_t rW2() const { return mkrsrc(w2_ + cb * 128, (unsigned)(DFF * D_ - cb * 128) * 2u); }
    __device__ __forceinline__ rsrc_t rWo() const { return mkrsrc(wo_ + cb * 128, (unsigned)(D_ * D_ - cb * 128) * 2u); } };
__device__ __forceinline__ G2 g2_setup(const bf16_t* ao, const bf16_t* w2, const bf16_t* wo, int rb, int cb, int kh, int split) {
    const int lane = threadIdx.x & 63, wid = __builtin_amdgcn_readfirstlane(threadIdx.x >> 6);
    G2 s; s.kh = kh; s.k0 = kh ? split : 0; s.nkt = kh ? 72 - split : split; s.rb = rb; s.cb = cb; s.ao_ = ao; s.w2_ = w2; s.wo_ = wo;
    s.aoff = (unsigned)((16 * wid + (lane >> 3)) * LDAO + (((lane & 7) ^ ((lane >> 3) & 7)) * 8)) * 2u;
#pragma unroll
    for (int i = 0; i < 2; ++i) { const int k = 8 * wid + 4 * i + (lane >> 4), cp = lane & 15, swz = 2 * ((k & 3) | (((k >> 3) & 1) << 2)); s.boff[i] = (unsigned)(k * D_ + (cp ^ swz) * 8) * 2u; }
    return s;
}
__device__ __forceinline__ int g2_slot(int t) { return (int)((0x10002030u >> (8 * (t & 3))) & 0xffu) * 2048; }
__device__ __forceinline__ void g2_stage(lds_ptr lds, const G2& s, int t, int sl) {
    const int wid = __builtin_amdgcn_readfirstlane(threadIdx.x >> 6), gt = s.k0 + t; const lds_ptr d = lds + g2_slot(sl);
#pragma unroll
    for (int i = 0; i < 2; ++i) bl_lds_dev(s.rA(), d + (16 * wid + 8 * i) * 128, s.aoff, t * 128 + i * (8 * LDAO * 2));
    if (gt < 56) {
#pragma unroll
        for (int i = 0; i < 2; ++i) bl_lds(s.rW2(), d + 16384 + (8 * wid + 4 * i) * 256, s.boff[i], gt * (64 * D_ * 2));
    } else {
#pragma unroll
        for (int i = 0; i < 2; ++i) bl_lds(s.rWo(), d + 16384 + (8 * wid + 4 * i) * 256, s.boff[i], (gt - 56) * (64 * D_ * 2));
    }
}

constexpr int AT_KT = 128, AT_STAGE = 4 * AT_KT * 64, AT_NST = 1024 / AT_KT, AT_Q = 122880, AT_FL = 139264;
__device__ __forceinline__ void attn_body(lds_ptr lds, const bf16_t* qkv, bf16_t* ao, unsigned* flags, const unsigned T, const int head, const int qb, const int hpub, const int hwait, const G2& g2) {
    const int tid = threadIdx.x, lane = tid & 63, wid = __builtin_amdgcn_readfirstlane(tid >> 6), wq = wid & 3, kh = wid >> 2;
    const int r32 = lane & 31, hi = lane >> 5;
    const rsrc_t rs = mkrsrc(qkv, 3 * S_ * D_ * 2);
    const unsigned ssrc = (unsigned)((((1 + (wid & 1)) * H_ + head) * S_ + (wid >> 1) * 1024) * 64);
    auto stage = [&](int st, int slot) {
#pragma unroll
        for (int i = 0; i < 8; ++i) bl_lds_dev(rs, lds + slot * AT_STAGE + (wid & 3) * 8192 + i * 1024, lane * 16, ssrc + st * (AT_KT * 64) + i * 1024); };
    if (wid < 4) {
        poll_tags(flags, F_Q + (((lane >> 3) & 3) * 1024 + head * 32) / 192 * 32 + (lane & 7), T, 24);
#pragma unroll
        for (int i = 0; i < 4; ++i) bl_lds_dev(rs, lds + AT_Q + wid * 4096 + i * 1024, lane * 16, (unsigned)((head * S_ + qb * 256) * 64 + wid * 4096 + i * 1024));
    }
    const int kr[2] = {r32 * 64 + ((hi ^ ((r32 >> 2) & 3)) * 16), r32 * 64 + (((2 + hi) ^ ((r32 >> 2) & 3)) * 16)};
    const int vr = AT_KT * 64 + (4 * hi + ((lane & 15) >> 2)) * 64 + ((lane >> 4) & 1) * 32 + (lane & 3) * 8;
    bf16x8 sel;
    { const short one = ((lane & 15) == ((lane >> 4) & 1)) ? (short)0x3f80 : (short)0;
#pragma unroll
      for (int j = 0; j < 8; ++j) sel[j] = one; }
    bf16x8 qf[2][2];
    float mref[2];
    f32x16 o[2]; f32x4 ls[2];
    for (int attempt = 0; attempt < 2; ++attempt) {
        if (wid < 4) { stage(0, 0); stage(1, 1); asm volatile("s_waitcnt vmcnt(8)" ::: "memory"); WAIT_BAR(8); } else LGKM_BAR();
        if (attempt == 0) {
#pragma unroll
            for (int a = 0; a < 2; ++a)
#pragma unroll
                for (int s = 0; s < 2; ++s) qf[a][s] = lds_b128(lds + AT_Q + (wq * 64 + a * 32 + r32) * 64 + (2 * s + hi) * 16);
            const bf16x8 k0 = lds_b128(lds + kr[0]), k1 = lds_b128(lds + kr[1]);
#pragma unroll
            for (int a = 0; a < 2; ++a) { f32x16 s = {}; s = MFMA32(k0, qf[a][0], s); s = MFMA32(k1, qf[a][1], s);
                float m = s[0];
#pragma unroll
                for (int r = 1; r < 16; ++r) m = fmaxf(m, s[r]);
                auto sw = __builtin_amdgcn_permlane32_swap(__float_as_uint(m), __float_as_uint(m), false, false);
                mref[a] = fmaxf(__uint_as_float(sw[0]), __uint_as_float(sw[1])); }
        }
        f32x16 nm[2];
#pragma unroll
        for (int a = 0; a < 2; ++a) {
#pragma unroll
            for (int r = 0; r < 16; ++r) nm[a][r] = -mref[a];
            o[a] = (f32x16){}; ls[a] = (f32x4){}; }
        bf16x8 kA[2], kB[2]; s16x4 vA[4], vB[4]; f32x16 sa_, sb_; u32x4 pa0, pa1, pb0, pb1;
        { const lds_ptr base = lds + kh * (2 * AT_KT * 64);
          kA[0] = lds_b128(base + kr[0]); kA[1] = lds_b128(base + kr[1]);
          sa_ = MFMA32(kA[0], qf[0][0], nm[0]); sa_ = MFMA32(kA[1], qf[0][1], sa_);
          pb0 = (u32x4){0u, 0u, 0u, 0u}; pb1 = pb0;
#pragma unroll
          for (int i = 0; i < 4; ++i) vB[i] = (s16x4){0, 0, 0, 0}; }
#define EXP2(S, i) S[i] = __builtin_amdgcn_exp2f(S[i])
#define PF(x) __builtin_bit_cast(bf16x8, x)
#define ASTEP(SC, PC0, PC1, SN, KF, QN, NMN, ON, LN_, VF, PP0, PP1, R0, R1, R2, R3, R4, R5) do { \
            SN = MFMA32(KF[0], QN[0], NMN); SB0(); EXP2(SC, 0); EXP2(SC, 1); EXP2(SC, 2); R0; SB0(); \
            SN = MFMA32(KF[1], QN[1], SN); SB0(); EXP2(SC, 3); EXP2(SC, 4); EXP2(SC, 5); PC0[0] = cvtpk(SC[0], SC[1]); R1; SB0(); \
            ON = MFMA32(cat4(VF[0], VF[1]), PF(PP0), ON); SB0(); EXP2(SC, 6); EXP2(SC, 7); EXP2(SC, 8); PC0[1] = cvtpk(SC[2], SC[3]); R2; SB0(); \
            ON = MFMA32(cat4(VF[2], VF[3]), PF(PP1), ON); SB0(); EXP2(SC, 9); EXP2(SC, 10); EXP2(SC, 11); PC0[2] = cvtpk(SC[4], SC[5]); R3; SB0(); \
            LN_ = MFMA16(sel, PF(PP0), LN_); SB0(); EXP2(SC, 12); EXP2(SC, 13); PC0[3] = cvtpk(SC[6], SC[7]); PC1[0] = cvtpk(SC[8], SC[9]); R4; SB0(); \
            LN_ = MFMA16(sel, PF(PP1), LN_); SB0(); EXP2(SC, 14); EXP2(SC, 15); PC1[1] = cvtpk(SC[10], SC[11]); PC1[2] = cvtpk(SC[12], SC[13]); R5; PC1[3] = cvtpk(SC[14], SC[15]); asm volatile("" : "+v"(PC0), "+v"(PC1)); SB0(); } while (0)
#define ATILE(KC, VP, KN, VN, kb, vb) do { \
            ASTEP(sa_, pa0, pa1, sb_, KC, qf[1], nm[1], o[1], ls[1], VP, pb0, pb1, KN[0] = lds_b128((kb) + kr[0]), KN[1] = lds_b128((kb) + kr[1]), \
                  VN[0] = lds_tr((vb) + vr), VN[1] = lds_tr((vb) + vr + 512), VN[2] = lds_tr((vb) + vr + 1024), VN[3] = lds_tr((vb) + vr + 1536)); \
            ASTEP(sb_, pb0, pb1, sa_, KN, qf[0], nm[0], o[0], ls[0], VN, pa0, pa1, (void)0, (void)0, (void)0, (void)0, (void)0, (void)0); } while (0)
        const unsigned aev = wid >= 4 ? 0x54u : (0xc0u | (wid == 0 ? 0x24u : 0u) | ((wid == 1 && blockIdx.x == 0) ? 0x10u : 0u));
        int slot = 0;
        for (int st = 0; st < AT_NST; ++st) {
            const int nslot = (slot == 2) ? 0 : slot + 1;
            const lds_ptr base = lds + slot * AT_STAGE + kh * (2 * AT_KT * 64), nbase = lds + nslot * AT_STAGE + kh * (2 * AT_KT * 64);
            SB0();
            ATILE(kA, vB, kB, vA, base + 2048, base);
            ATILE(kB, vA, kA, vB, base + 4096, base + 2048);
            ATILE(kA, vB, kB, vA, base + 6144, base + 4096);
            if (__builtin_expect((aev >> st) & 1u, 0)) {
                if (wid < 4) {
                    if (attempt == 0 && st == 5 && wid == 0) poll_tags(flags, hwait + lane, T, 16);
                    if (attempt == 0 && st == 4) {
#pragma unroll 1
                        for (int i = 0; i < 4; ++i) poll_tags(flags, F_ST + 64 * i + lane, T, 64);
                        if (lane == 0) __hip_atomic_store(flags + F_E, T, RLX_AGENT); }
                    WAIT_BAR(0);
                    if (st + 2 < AT_NST) stage(st + 2, (slot == 0) ? 2 : slot - 1);
                } else {
                    WAIT_BAR(0);
                    if (attempt == 0 && st < 6) {
                        const int w = (blockIdx.x >> 3) * 4 + wid - 4; const bool second = st == 4;
                        const int n = g2.kh ? (second ? 8 : 10) : 9, k0 = g2.kh ? (second ? w * 8 : 2304 + w * 10) : w * 18 + (second ? 9 : 0);
                        const rsrc_t rp = mkrsrc(((g2.kh && second) ? g2.wo_ : g2.w2_) + (g2.cb >> 2) * 512, (DFF * D_ - 512) * 2);
                        for (int i = 0; i < n; ++i) bl_lds(rp, lds + JUNK, lane * 16, (k0 + i) * (D_ * 2)); } }
                if (attempt == 0 && st == 2 && hpub >= 0) publish(flags, hpub, T);
                if (attempt == 0 && st == AT_NST - 2) g2_stage(lds, g2, 0, 0);
            } else if (wid < 4) { WAIT_BAR(0); stage(st + 2, (slot == 0) ? 2 : slot - 1); }
            else LGKM_BAR();
            SB0();
            ATILE(kB, vA, kA, vB, nbase, base + 6144);
            slot = nslot;
        }
        o[1] = MFMA32(cat4(vB[0], vB[1]), PF(pb0), o[1]); o[1] = MFMA32(cat4(vB[2], vB[3]), PF(pb1), o[1]);
        ls[1] = MFMA16(sel, PF(pb0), ls[1]); ls[1] = MFMA16(sel, PF(pb1), ls[1]);
#undef ATILE
#undef ASTEP
#undef EXP2
#undef PF
        LGKM_BAR();
        if (attempt == 1) break;
        bool bad = false;
#pragma unroll
        for (int a = 0; a < 2; ++a) { bad |= !(ls[a][0] < 1.0e18f) || !(ls[a][1] < 1.0e18f);
#pragma unroll
            for (int r = 0; r < 16; ++r) bad |= !(fabsf(o[a][r]) < 1.0e30f); }
        LAS unsigned* fl = (LAS unsigned*)(lds + AT_FL);
        if (lane == 0) fl[wid] = __any(bad) ? 1u : 0u;
        LGKM_BAR();
        const u32x4 f0 = *(const LAS u32x4*)fl, f1 = *(const LAS u32x4*)(fl + 4);
        if ((f0[0] | f0[1] | f0[2] | f0[3] | f1[0] | f1[1] | f1[2] | f1[3]) == 0u) break;
        float mx[2] = {-INFINITY, -INFINITY};
        for (int st = 0; st < AT_NST; ++st) {
            if (wid < 4) stage(st, 0);
            WAIT_BAR(0);
#pragma unroll
            for (int h2 = 0; h2 < 2; ++h2)
                for (int kt = 0; kt < AT_KT / 32; ++kt) {
                    const lds_ptr base = lds + h2 * (2 * AT_KT * 64) + kt * 2048;
                    const bf16x8 k0 = lds_b128(base + kr[0]), k1 = lds_b128(base + kr[1]);
#pragma unroll
                    for (int a = 0; a < 2; ++a) { f32x16 s = {}; s = MFMA32(k0, qf[a][0], s); s = MFMA32(k1, qf[a][1], s);
#pragma unroll
                        for (int r = 0; r < 16; ++r) mx[a] = fmaxf(mx[a], s[r]); }
                }
            LGKM_BAR();
        }
#pragma unroll
        for (int a = 0; a < 2; ++a) { auto sw = __builtin_amdgcn_permlane32_swap(__float_as_uint(mx[a]), __float_as_uint(mx[a]), false, false);
            mref[a] = fmaxf(__uint_as_float(sw[0]), __uint_as_float(sw[1])); }
    }
    g2_stage(lds, g2, 1, 1);
    LAS float* xo = (LAS float*)lds + wq * (2 * 17 * 64);
    if (kh == 1) {
#pragma unroll
        for (int a = 0; a < 2; ++a) {
#pragma unroll
            for (int r = 0; r < 16; ++r) xo[(a * 17 + r) * 64 + lane] = o[a][r];
            if (lane < 16) { xo[(a * 17 + 16) * 64 + lane] = ls[a][0]; xo[(a * 17 + 16) * 64 + 16 + lane] = ls[a][1]; } }
    } else {
#pragma unroll
        for (int a = 0; a < 2; ++a) if (lane < 16) { xo[(a * 17 + 16) * 64 + 32 + lane] = ls[a][0]; xo[(a * 17 + 16) * 64 + 48 + lane] = ls[a][1]; }
    }
    LGKM_BAR();
    if (kh == 0) {
        const rsrc_t rsa = mkrsrc(ao, S_ * LDAO * 2);
#pragma unroll
        for (int a = 0; a < 2; ++a) {
            const float l = xo[(a * 17 + 16) * 64 + r32] + xo[(a * 17 + 16) * 64 + 32 + r32];
            const float rl = 1.0f / l;
            unsigned pk[8];
#pragma unroll
            for (int r = 0; r < 8; ++r) pk[r] = cvtpk((o[a][2 * r] + xo[(a * 17 + 2 * r) * 64 + lane]) * rl, (o[a][2 * r + 1] + xo[(a * 17 + 2 * r + 1) * 64 + lane]) * rl);
            const int dst = (qb * 256 + wq * 64 + a * 32 + r32) * LDAO + DFF + head * DH;
#pragma unroll
            for (int k = 0; k < 4; k += 2) {
                auto r0 = __builtin_amdgcn_permlane32_swap(pk[2 * k], pk[2 * k + 2], false, false); auto r1 = __builtin_amdgcn_permlane32_swap(pk[2 * k + 1], pk[2 * k + 3], false, false);
                st_wt((u32x4){r0[0], r1[0], r0[1], r1[1]}, rsa, (unsigned)(dst + 8 * k + 8 * hi) * 2u);
            }
        }
    }
    LGKM_BAR();
}

__device__ __forceinline__ void gemm2_body(lds_ptr lds, const G2& g2, const bf16_t* __restrict__ x, bf16_t* __restrict__ out, float* part, unsigned* flags, const unsigned T, const int rb, const int cb, const int kh, const int opub) {
    const int NKT = g2.nkt;
    const int tid = threadIdx.x, lane = tid & 63, wid = __builtin_amdgcn_readfirstlane(tid >> 6), kg = wid >> 2, wm = (wid >> 1) & 1, wn = wid & 1;
    const int fr = lane & 15, g = lane >> 4, q = fr >> 2, p = lane & 3;
    const int m0 = rb * 128, n0 = cb * 128;
    const int ar = (64 * wm + fr) * 128 + (((4 * kg + g) ^ (fr & 7)) * 16);
    int br[4];
    { const int swz = 2 * (q | ((g & 1) << 2));
#pragma unroll
      for (int j = 0; j < 4; ++j) br[j] = 16384 + (32 * kg + 8 * g + q) * 256 + (((2 * (4 * wn + j) + (p >> 1)) ^ swz) * 16) + (p & 1) * 8; }
    f32x4 acc[4][4];
#pragma unroll
    for (int i = 0; i < 4; ++i)
#pragma unroll
        for (int j = 0; j < 4; ++j) acc[i][j] = (f32x4){0.f, 0.f, 0.f, 0.f};
    bf16x8 A0[4], A1[4]; s16x4 B0[4][2], B1[4][2];
    const int wid4 = wid; const rsrc_t rA2 = g2.rA(), rW2 = g2.rW2(), rWo = g2.rWo();
    auto st1 = [&](int t, int sl, int i) {
        const int gt = g2.k0 + t; const lds_ptr d = lds + g2_slot(sl);
        if (i < 2) dma16<true, 0>(rA2, d + (16 * wid4 + 8 * i) * 128, g2.aoff, t * 128 + i * (8 * LDAO * 2));
        else { const bool w = gt < 56; dma16<false, 0>(w ? rW2 : rWo, d + 16384 + (8 * wid4 + 4 * (i - 2)) * 256, g2.boff[i - 2], (w ? gt : gt - 56) * (64 * D_ * 2)); } };
#define RDONE(AN, BN_, so, r) do { if ((r) < 4) AN[(r) & 3] = lds_b128(lds + (so) + ar + ((r) & 3) * 2048); \
        else if ((r) < 12) BN_[(((r) - 4) >> 1) & 3][(r) & 1] = lds_tr(lds + (so) + br[(((r) - 4) >> 1) & 3] + ((r) & 1) * 1024); } while (0)
#define STEP(AC, BC, AN, BN_, so, tl, sl) do { \
        unroll<0, 16>([&](auto ic) { constexpr int idx = decltype(ic)::value; constexpr int j = idx >> 2, i = idx & 3; \
            acc[i][j] = MFMA16(cat4(BC[j][0], BC[j][1]), AC[i], acc[i][j]); SB0(); \
            RDONE(AN, BN_, so, idx); \
            if ((idx & 3) == 2) st1(tl, sl, idx >> 2); \
            SB0(); }); } while (0)
    g2_stage(lds, g2, 2, 2); g2_stage(lds, g2, 3, 3);
    WAIT_BAR(8);
    publish(flags, opub, T);
    unroll<0, 12>([&](auto ic) { constexpr int idx = decltype(ic)::value; RDONE(A0, B0, g2_slot(0), idx); });
    WAIT_BAR(8);
    for (int t = 0; t < NKT; t += 2) {
        if (kh == 1 && t == 48 - g2.k0 && wid == 0) poll_tags(flags, F_O + (rb >> 1) * 32 + (lane & 31), T, 32);
        SB0();
        STEP(A0, B0, A1, B1, g2_slot(t + 1), min(t + 4, NKT - 1), t);
        WAIT_BAR(8); SB0();
        STEP(A1, B1, A0, B0, g2_slot(t + 2), min(t + 5, NKT - 1), t + 1);
        WAIT_BAR(8);
    }
#undef RDONE
#undef STEP
    WAIT_BAR(0);
    const int mrow = m0 + 64 * wm + 32 * kg + 16 * kh + fr;
    u32x2 xv[4];
#pragma unroll
    for (int j = 0; j < 4; ++j) xv[j] = *(const u32x2*)(x + (size_t)mrow * D_ + n0 + 64 * wn + 16 * j + 4 * g);
    LAS f32x4* xr = (LAS f32x4*)lds + (wid & 3) * 1024;
#pragma unroll
    for (int i = 0; i < 2; ++i)
#pragma unroll
        for (int j = 0; j < 4; ++j) xr[(kg * 8 + i * 4 + j) * 64 + lane] = kg ? acc[i][j] : acc[2 + i][j];
    LGKM_BAR();
    f32x4 r[2][4];
#pragma unroll
    for (int i = 0; i < 2; ++i)
#pragma unroll
        for (int j = 0; j < 4; ++j) r[i][j] = (kg ? acc[2 + i][j] : acc[i][j]) + xr[((kg ^ 1) * 8 + i * 4 + j) * 64 + lane];
    const int tile = rb * 8 + cb;
    { const rsrc_t rs = mkrsrc(part + (size_t)(tile * 2 + kh) * 8192, 32768);
#pragma unroll
      for (int j = 0; j < 4; ++j) st_wt(__builtin_bit_cast(u32x4, kh ? r[0][j] : r[1][j]), rs, (wid * 4 + j) * 1024 + lane * 16); }
    asm volatile("s_waitcnt vmcnt(0)" ::: "memory");
    if (lane == 0) __hip_atomic_store(flags + F_P + (tile * 2 + kh) * 32 + wid, T, RLX_AGENT);
    poll_tags(flags, F_P + (tile * 2 + (kh ^ 1)) * 32 + wid, T, 1);
    { const rsrc_t rs = mkrsrc(part + (size_t)(tile * 2 + (kh ^ 1)) * 8192, 32768);
      f32x4 o[4];
#pragma unroll
      for (int j = 0; j < 4; ++j) o[j] = __builtin_bit_cast(f32x4, __builtin_amdgcn_raw_buffer_load_b128(rs, (wid * 4 + j) * 1024 + lane * 16, 0, 16));
      unsigned pk[4][2];
#pragma unroll
      for (int j = 0; j < 4; ++j) { f32x4 v = (kh ? r[1][j] : r[0][j]) + o[j];
          v[0] += __uint_as_float(xv[j][0] << 16); v[1] += __uint_as_float(xv[j][0] & 0xffff0000u); v[2] += __uint_as_float(xv[j][1] << 16); v[3] += __uint_as_float(xv[j][1] & 0xffff0000u);
          pk[j][0] = cvtpk(v[0], v[1]); pk[j][1] = cvtpk(v[2], v[3]); }
#pragma unroll
      for (int j = 0; j < 4; j += 2) {
          auto r0 = __builtin_amdgcn_permlane16_swap(pk[j][0], pk[j + 1][0], false, false); auto r1 = __builtin_amdgcn_permlane16_swap(pk[j][1], pk[j + 1][1], false, false);
          *(u32x4*)(out + (size_t)mrow * D_ + n0 + 64 * wn + 16 * (j + (g & 1)) + 8 * (g >> 1)) = (u32x4){r0[0], r1[0], r0[1], r1[1]}; } }
}

__global__ __launch_bounds__(512, 2) void fused_kernel(const bf16_t* __restrict__ wqkv, const bf16_t* __restrict__ w1, const bf16_t* __restrict__ w2, const bf16_t* __restrict__ wo,
                                                       const bf16_t* __restrict__ x, bf16_t* qkv, bf16_t* ao, bf16_t* __restrict__ out, float* part, unsigned* flags) {
    __shared__ __attribute__((aligned(16))) char smem[LDS_BYTES];
    const lds_ptr lds = (lds_ptr)smem;
    const int xg = blockIdx.x & 7, r = blockIdx.x >> 3;
    {
        unsigned long long pc; unsigned len;
        asm volatile("s_getpc_b64 %0\n.Lfk_beg:\n\ts_mov_b32 %1, .Lfk_end-.Lfk_beg" : "=s"(pc), "=s"(len));
        if (threadIdx.x >= 448) { const rsrc_t rc = mkrsrc((const void*)pc, len);
            bl_lds(rc, lds + JUNK, (threadIdx.x & 63) * 16, r * 2048); bl_lds(rc, lds + JUNK, (threadIdx.x & 63) * 16, r * 2048 + 1024); }
    }
    const int rb1 = (xg & 1) * 4 + (r & 3), c1 = (xg >> 1) * 4 + ((r >> 2) & 3), ffn = r >> 4;
    unsigned T;
    if (ffn) T = gemm1_body<7>(lds, x, w1, DFF, rb1 * 256, c1 * 224, qkv, ao, flags, 16 + c1, wqkv, w1);
    else { T = gemm1_body<6>(lds, x, wqkv, 3 * D_, rb1 * 256, c1 * 192, qkv, ao, flags, c1, wqkv, w1); publish(flags, F_Q + c1 * 32 + rb1, T); }
    const int kh = xg & 1, rr = kh ? (r ^ 16) : r, rb3 = ((xg >> 1) & 1) * 8 + (rr & 7), cb3 = (xg >> 2) * 4 + (rr >> 3);
    const G2 g2 = g2_setup(ao, w2, wo, rb3, cb3, kh, (rr >> 4) ? 72 - SPLITQ : SPLITQ);
    const int head = xg * 4 + (r >> 3), qb = r & 7;
    attn_body(lds, qkv, ao, flags, T, head, qb, ffn ? F_H + rb1 * 32 + c1 : -1, F_H + (rb3 >> 1) * 32, g2);
    gemm2_body(lds, g2, x, out, part, flags, T, rb3, cb3, kh, F_O + qb * 32 + head);
    asm volatile(".Lfk_end:");
}

extern "C" void kernel_launch(void* const* d_in, const int* in_sizes, int n_in, void* d_out, int out_size, void* d_ws, size_t ws_size, hipStream_t stream) {
    const bf16_t* x = (const bf16_t*)d_in[0]; const bf16_t* wqkv = (const bf16_t*)d_in[1]; const bf16_t* wo = (const bf16_t*)d_in[2];
    const bf16_t* w1 = (const bf16_t*)d_in[4]; const bf16_t* w2 = (const bf16_t*)d_in[5];
    char* ws = (char*)d_ws;
    unsigned* flags = (unsigned*)(ws + WS_FLAGS); bf16_t* qkv = (bf16_t*)(ws + WS_QKV); bf16_t* ao = (bf16_t*)(ws + WS_AO); float* part = (float*)(ws + WS_PART);
    fused_kernel<<<256, 512, 0, stream>>>(wqkv, w1, w2, wo, x, qkv, ao, (bf16_t*)d_out, part, flags);
}
```

```cpp
#include <hip/hip_runtime.h>
#include <hip/hip_bf16.h>
#include <stdint.h>

typedef unsigned short bf16_t;
typedef short bf16x8 __attribute__((ext_vector_type(8)));
typedef short s16x4 __attribute__((ext_vector_type(4)));
typedef float f32x2 __attribute__((ext_vector_type(2)));
typedef float f32x4 __attribute__((ext_vector_type(4)));
typedef float f32x16 __attribute__((ext_vector_type(16)));
typedef unsigned u32x4 __attribute__((ext_vector_type(4)));
typedef unsigned u32x2 __attribute__((ext_vector_type(2)));
typedef __bf16 bf16x2n __attribute__((ext_vector_type(2)));
#define LAS __attribute__((address_space(3)))
typedef LAS char* lds_ptr;

constexpr int S_ = 2048, D_ = 1024, H_ = 32, DH = 32, DFF = 3584;
constexpr int LDAO = DFF + D_;
constexpr size_t WS_FLAGS = 0;
constexpr size_t WS_QKV = 1u << 20;
constexpr size_t WS_AO = WS_QKV + (size_t)3 * S_ * D_ * 2;
constexpr size_t WS_PART = WS_AO + (size_t)S_ * LDAO * 2;
constexpr int LDS_BYTES = 163840;
constexpr int F_E = 0, F_ST = 256, F_Q = 1024, F_H = 2048, F_O = 3072, F_P = 4096;
constexpr int F_LT = 512, F_CT = 768;
constexpr int F_LN = 16384, F_CS = 32768;
constexpr int NCOL = 3 * D_ + DFF;
constexpr int SPLITQ = 40;
constexpr int TBL = 151552;


__device__ __forceinline__ unsigned cvtpk(float lo, float hi) { return __builtin_bit_cast(unsigned, __builtin_convertvector((f32x2){lo, hi}, bf16x2n)); }
__device__ __forceinline__ float rbf(float x) { return __uint_as_float(cvtpk(x, x) << 16); }
typedef __amdgpu_buffer_rsrc_t rsrc_t;
__device__ __forceinline__ rsrc_t mkrsrc(const void* p, unsigned bytes) { return __builtin_amdgcn_make_buffer_rsrc((void*)p, 0, bytes, 0x00020000); }
template <bool DEV, int NOP> __device__ __forceinline__ void dma16(rsrc_t r, lds_ptr l, unsigned voff, unsigned soff) {
    const unsigned a = __builtin_amdgcn_readfirstlane((unsigned)(uintptr_t)l), so = __builtin_amdgcn_readfirstlane(soff);
    if (DEV) asm volatile("s_mov_b32 m0, %0\n\ts_nop %4\n\tbuffer_load_dwordx4 %1, %2, %3 offen sc1 lds" :: "s"(a), "v"(voff), "s"(r), "s"(so), "n"(NOP) : "memory");
    else asm volatile("s_mov_b32 m0, %0\n\ts_nop %4\n\tbuffer_load_dwordx4 %1, %2, %3 offen lds" :: "s"(a), "v"(voff), "s"(r), "s"(so), "n"(NOP) : "memory"); }
__device__ __forceinline__ void bl_lds(rsrc_t r, lds_ptr l, unsigned voff, unsigned soff) { dma16<false, 3>(r, l, voff, soff); }
__device__ __forceinline__ void bl_lds_dev(rsrc_t r, lds_ptr l, unsigned voff, unsigned soff) { dma16<true, 3>(r, l, voff, soff); }
__device__ __forceinline__ void st_wt(u32x4 v, rsrc_t r, unsigned off) { __builtin_amdgcn_raw_buffer_store_b128(v, r, off, 0, 16); }
__device__ __forceinline__ bf16x8 lds_b128(lds_ptr p) { return *(const LAS bf16x8*)p; }
__device__ __forceinline__ s16x4 lds_tr(lds_ptr p) { return __builtin_amdgcn_ds_read_tr16_b64_v4i16((LAS s16x4*)p); }
__device__ __forceinline__ bf16x8 cat4(s16x4 a, s16x4 b) { return (bf16x8){a[0], a[1], a[2], a[3], b[0], b[1], b[2], b[3]}; }
#define MFMA16(a, b, c) __builtin_amdgcn_mfma_f32_16x16x32_bf16(a, b, c, 0, 0, 0)
#define MFMA32(a, b, c) __builtin_amdgcn_mfma_f32_32x32x16_bf16(a, b, c, 0, 0, 0)
#define WAIT_BAR(N) asm volatile("s_waitcnt vmcnt(%0) lgkmcnt(0)\n\ts_barrier" :: "n"(N) : "memory")
#define LGKM_BAR() asm volatile("s_waitcnt lgkmcnt(0)\n\ts_barrier" ::: "memory")
#define SB0() __builtin_amdgcn_sched_barrier(0)
#define RLX_AGENT __ATOMIC_RELAXED, __HIP_MEMORY_SCOPE_AGENT
template <int I> struct IC { static constexpr int value = I; };
template <int B, int E, class F> __device__ __forceinline__ void unroll(F&& f) { if constexpr (B < E) { f(IC<B>{}); unroll<B + 1, E>(f); } }

__device__ __forceinline__ float wave_sum(float v) {
    v += __builtin_bit_cast(float, __builtin_amdgcn_update_dpp(0, __builtin_bit_cast(int, v), 0xB1, 0xf, 0xf, true));
    v += __builtin_bit_cast(float, __builtin_amdgcn_update_dpp(0, __builtin_bit_cast(int, v), 0x4E, 0xf, 0xf, true));
    v += __builtin_bit_cast(float, __builtin_amdgcn_update_dpp(0, __builtin_bit_cast(int, v), 0x141, 0xf, 0xf, true));
    v += __builtin_bit_cast(float, __builtin_amdgcn_update_dpp(0, __builtin_bit_cast(int, v), 0x140, 0xf, 0xf, true));
    { auto r = __builtin_amdgcn_permlane16_swap(__float_as_uint(v), __float_as_uint(v), false, false); v = __uint_as_float(r[0]) + __uint_as_float(r[1]); }
    { auto r = __builtin_amdgcn_permlane32_swap(__float_as_uint(v), __float_as_uint(v), false, false); v = __uint_as_float(r[0]) + __uint_as_float(r[1]); }
    return v;
}
__device__ __forceinline__ void poll_tags(unsigned* flags, int word, unsigned T, int n) {
    const bool act = (int)(threadIdx.x & 63) < n; unsigned spins = 0;
    for (;;) { unsigned v = T; if (act) v = __hip_atomic_load(flags + word, RLX_AGENT);
        if (__all(v == T)) break; __builtin_amdgcn_s_sleep(2); if (++spins > (1u << 22)) break; }
    asm volatile("" ::: "memory");
}
__device__ __forceinline__ void publish(unsigned* flags, int word, unsigned T) {
    if (threadIdx.x == 0) __hip_atomic_store(flags + word, T, RLX_AGENT);
}

constexpr int JUNK = 162816;
constexpr int HSTG = 65536;
template <int NT>
__device__ __forceinline__ unsigned gemm1_body(lds_ptr lds, const bf16_t* xn, const bf16_t* W, const int ldw, const int m0, const int n0, bf16_t* qkv, bf16_t* ao, unsigned* flags, const int j32, const bf16_t* wq_, const bf16_t* w1_) {
    constexpr int BN = 32 * NT, RB = BN * 2, CPR = BN / 8, BSLOT = 64 * RB, NKT = D_ / 64, GB = (NT == 7) ? 4 : 3;
    constexpr int LB = 65536, NMF = 4 * NT, NRD = 4 + 2 * NT, NBB = (NT == 7) ? 2 : 4;
    const int tid = threadIdx.x, lane = tid & 63, wid = __builtin_amdgcn_readfirstlane(tid >> 6), wm = wid & 3, wn = wid >> 2;
    const int fr = lane & 15, g = lane >> 4, q = fr >> 2, p = lane & 3;
    const rsrc_t rA = mkrsrc(xn + (size_t)m0 * D_, 256 * D_ * 2), rB = mkrsrc(W + n0, (unsigned)(D_ * ldw - n0) * 2u);
    const unsigned aoff = (unsigned)((32 * wid + (lane >> 3)) * D_ + (((lane & 7) ^ ((lane >> 3) & 7)) * 8)) * 2u;
    unsigned boff[GB];
#pragma unroll
    for (int i = 0; i < GB; ++i) { const int idx = 64 * i + lane, rl = idx / CPR, cp = idx % CPR, k = (8 * wid + rl) & 63;
        const int swz = (NT == 7) ? 2 * ((k >> 3) & 1) : 2 * (((k >> 1) & 1) | (((k >> 3) & 1) << 1));
        boff[i] = (unsigned)(k * ldw + (cp ^ swz) * 8) * 2u; }
    const int krot = (m0 >> 10) * 8, tcs = ((m0 >> 8) & 3) * 2;
    auto stageA1 = [&](int kt_, int slot, int i) { const int kt = (kt_ + krot) & (NKT - 1); dma16<false, 0>(rA, lds + slot * 32768 + (32 * wid + 8 * i) * 128, aoff, kt * 128 + i * (8 * D_ * 2)); };
    auto stageB1 = [&](int kt_, int slot, int i) { const int kt = (kt_ + krot) & (NKT - 1); const unsigned so = (unsigned)(kt * 64 * ldw) * 2u;
        if (NT == 7 && i == 3) { if (lane < 32) dma16<false, 0>(rB, lds + LB + slot * BSLOT + (8 * wid * CPR + 192) * 16, boff[GB - 1], so); }
        else dma16<false, 0>(rB, lds + LB + slot * BSLOT + (8 * wid * CPR + 64 * i) * 16, boff[i < GB ? i : 0], so); };
    int ar[2];
#pragma unroll
    for (int ks = 0; ks < 2; ++ks) ar[ks] = (64 * wm + fr) * 128 + (((4 * ks + g) ^ (fr & 7)) * 16);
    int br[NBB];
    { const int swz = (NT == 7) ? 2 * (g & 1) : 2 * ((q >> 1) | ((g & 1) << 1));
#pragma unroll
      for (int j = 0; j < NBB; ++j) br[j] = LB + (8 * g + q) * RB + (((2 * (NT * wn + j) + (p >> 1)) ^ swz) * 16) + (p & 1) * 8; }
    f32x4 acc[4][NT];
#pragma unroll
    for (int i = 0; i < 4; ++i)
#pragma unroll
        for (int j = 0; j < NT; ++j) acc[i][j] = (f32x4){0.f, 0.f, 0.f, 0.f};
    bf16x8 A0[4], A1[4]; s16x4 B0[NT][2], B1[NT][2];
#define RDONE(AN, BN_, sa, sb, ks, r) do { if ((r) < 4) AN[(r) < 4 ? (r) : 0] = lds_b128(lds + (sa) + ar[ks] + ((r) < 4 ? (r) : 0) * 2048); \
        else if ((r) < NRD) { constexpr int jj = (((r) - 4) >> 1) < NT && (r) >= 4 ? (((r) - 4) >> 1) : 0, tt = ((r) - 4) & 1; BN_[jj][tt] = lds_tr(lds + (sb) + br[jj % NBB] + 32 * (jj - jj % NBB) + (32 * (ks) + 4 * tt) * RB); } } while (0)
#define SUBSTEP(AC, BC, AN, BN_, sa, sb, ks, STG, go, tl, sl) do { \
        unroll<0, NMF>([&](auto ic) { constexpr int idx = decltype(ic)::value; constexpr int j = idx >> 2, i = idx & 3; \
            acc[i][j] = MFMA16(cat4(BC[j][0], BC[j][1]), AC[i], acc[i][j]); SB0(); \
            RDONE(AN, BN_, sa, sb, ks, idx); \
            if ((idx % NT) == 1 && (go)) { if (STG == 1) stageA1(tl, sl, idx / NT); if (STG == 2 && idx / NT < GB) stageB1(tl, sl, idx / NT); } \
            SB0(); }); } while (0)
    unsigned T; float lnv;
    const int ncg = (NT == 7 ? 3 * D_ : 0) + n0;
    {   const int row = m0 + 8 * j32 + wid;
        const u32x4* src = (const u32x4*)(xn + (size_t)row * D_);
        u32x4 v0, v1; unsigned ev;
        asm volatile("global_load_dwordx4 %0, %3, off\n\tglobal_load_dwordx4 %1, %3, off offset:1024\n\tglobal_load_dword %2, %4, off sc1"
                     : "=&v"(v0), "=&v"(v1), "=&v"(ev) : "v"(src + lane), "v"(flags + F_E) : "memory");
#pragma unroll
        for (int t = 0; t < 2; ++t) {
#pragma unroll
            for (int i = 0; i < GB; ++i) stageB1(t, t, i);
#pragma unroll
            for (int i = 0; i < 4; ++i) stageA1(t, t, i); }
        asm volatile("s_waitcnt vmcnt(%3)" : "+v"(v0), "+v"(v1), "+v"(ev) : "n"(2 * (4 + GB)) : "memory");
        SB0();
        float f[16];
#pragma unroll
        for (int i = 0; i < 4; ++i) { f[2 * i] = __uint_as_float(v0[i] << 16); f[2 * i + 1] = __uint_as_float(v0[i] & 0xffff0000u); f[8 + 2 * i] = __uint_as_float(v1[i] << 16); f[8 + 2 * i + 1] = __uint_as_float(v1[i] & 0xffff0000u); }
        float sm = 0.f;
#pragma unroll
        for (int i = 0; i < 16; ++i) sm += f[i];
        const float mean = wave_sum(sm) * (1.0f / D_);
        float qq = 0.f;
#pragma unroll
        for (int i = 0; i < 16; ++i) { const float d = f[i] - mean; qq += d * d; }
        qq = wave_sum(qq);
        T = (unsigned)__builtin_amdgcn_readfirstlane(ev) + 1u;
        const float sd = rbf(__fsqrt_rn(rbf(rbf(qq * (1.0f / D_)) + 1e-5f)));
        lnv = (lane & 1) ? 1.0f / sd : rbf(mean);
    }
    WAIT_BAR(4 + GB);
    auto colsum = [&](s16x4 (&BC)[NT][2]) {
        const bf16x8 ones = {0x3f80, 0x3f80, 0x3f80, 0x3f80, 0x3f80, 0x3f80, 0x3f80, 0x3f80}; const f32x4 z = {0.f, 0.f, 0.f, 0.f};
        float c[NT];
#pragma unroll
        for (int j = 0; j < NT; ++j) c[j] = MFMA16(ones, cat4(BC[j][0], BC[j][1]), z)[0];
        if (lane < 16) {
#pragma unroll
            for (int j = 0; j < NT; ++j) ((LAS float*)(lds + TBL + 2048))[wm * 256 + 16 * (NT * wn + j) + lane] = c[j]; } };
    unroll<0, NRD>([&](auto ic) { constexpr int idx = decltype(ic)::value; RDONE(A0, B0, 0, 0, 0, idx); });
    int sa = 0, sb = 0, sb3 = 0;
    const rsrc_t rF = mkrsrc(flags, 1u << 20);
    const unsigned evmask = 1u | (1u << (tcs + (wm >> 1))) | (1u << (tcs + 3)) | (1u << (NKT - 2)) | (wid == 0 ? (4u | (1u << (tcs + 5)) | (1u << (NKT - 3))) : 0u);
    for (int t = 0; t < NKT; ++t) {
        const int tn = min(t + 2, NKT - 1); constexpr bool go = true;
        SB0();
        { const int slb = (sb3 == 0) ? 2 : sb3 - 1;
          SUBSTEP(A0, B0, A1, B1, sa, sb, 1, 2, go, tn, slb); }
        if (__builtin_expect((evmask >> t) & 1u, 0)) {
            if (t == 0) { if (lane < 2) __hip_atomic_store(flags + F_LN + (m0 + 8 * j32 + wid) * 2 + lane, __float_as_uint(lnv), RLX_AGENT);
                if (wid == 0) { publish(flags, F_ST + blockIdx.x, T); WAIT_BAR(GB + 2); } else WAIT_BAR(GB + 1); }
            else if (wid == 0 && (t == 2 || t == tcs + 5)) { publish(flags, (t == 2 ? F_LT : F_CT) + (m0 >> 8) * 32 + j32, T); WAIT_BAR(GB + 1); }
            else if (wid * 64 < BN && t == tcs + 3) { if (tid < BN) { const LAS float* pc = (const LAS float*)(lds + TBL + 2048) + tid;
                    __hip_atomic_store(flags + F_CS + (m0 >> 8) * NCOL + ncg + tid, __float_as_uint((pc[0] + pc[256]) + (pc[512] + pc[768])), RLX_AGENT); }
                WAIT_BAR(GB + 1); }
            else WAIT_BAR(GB);
            if (t == tcs + (wm >> 1)) { if (wm & 1) colsum(B1); else colsum(B0); }
            if (t == NKT - 3 && wid == 0) poll_tags(flags, lane < 32 ? F_LT + (m0 >> 8) * 32 + lane : F_CT + (lane & 7) * 32 + j32, T, 40);
            if (t == NKT - 2) {
                if (lane < BN / 4) bl_lds_dev(rF, lds + TBL + 2048 + wid * 1024, lane * 16, (F_CS + wid * NCOL + ncg) * 4);
                if (wid < 2) bl_lds_dev(rF, lds + TBL + wid * 1024, lane * 16, (F_LN + m0 * 2) * 4 + wid * 1024); }
        } else WAIT_BAR(GB);
        SB0();
        sa ^= 32768; sb3 = (sb3 == 2) ? 0 : sb3 + 1; sb = sb3 * BSLOT;
        SUBSTEP(A1, B1, A0, B0, sa, sb, 0, 1, go, tn, t & 1);
    }
#undef RDONE
#undef SUBSTEP
    if (tid < BN) { float cs = 0.f;
#pragma unroll
        for (int i = 0; i < 8; ++i) cs += ((const LAS float*)(lds + TBL + 2048))[i * 256 + tid];
        ((LAS float*)(lds + TBL + 10240))[tid] = cs; }
    WAIT_BAR(0);
#pragma unroll
    for (int i = 0; i < 4; ++i) { const f32x2 st = ((const LAS f32x2*)(lds + TBL))[64 * wm + 16 * i + fr];
#pragma unroll
        for (int j = 0; j < NT; ++j) { const f32x4 cs = *(const LAS f32x4*)(lds + TBL + 10240 + (16 * (NT * wn + j) + 4 * g) * 4); acc[i][j] = (acc[i][j] - cs * st[0]) * st[1]; } }
    const int mrow = m0 + 64 * wm + fr;
    if constexpr (NT == 6) {
        const rsrc_t rsq = mkrsrc(qkv, 3 * S_ * D_ * 2);
#pragma unroll
        for (int b = 0; b < 3; ++b) {
            const int col = n0 + 96 * wn + 32 * b, which = col >> 10, head = (col & 1023) >> 5;
            const float sc = (which == 0) ? 0.17677669529663687f * 1.4426950408889634f : 1.0f;
            const int dst = ((which * H_ + head) * S_) * DH;
#pragma unroll
            for (int i = 0; i < 4; ++i) {
                const f32x4 u = acc[i][2 * b] * sc, v = acc[i][2 * b + 1] * sc;
                unsigned x0 = cvtpk(u[0], u[1]), x1 = cvtpk(u[2], u[3]), y0 = cvtpk(v[0], v[1]), y1 = cvtpk(v[2], v[3]);
                auto r0 = __builtin_amdgcn_permlane16_swap(x0, y0, false, false); auto r1 = __builtin_amdgcn_permlane16_swap(x1, y1, false, false);
                const int tok = mrow + 16 * i; int ch = 2 * (g & 1) + (g >> 1); if (which == 1) ch ^= (tok >> 2) & 3;
                st_wt((u32x4){r0[0], r1[0], r0[1], r1[1]}, rsq, (unsigned)(dst + tok * DH + ch * 8) * 2u);
            }
        }
        WAIT_BAR(0);
    } else {
        const rsrc_t rsa = mkrsrc(ao, S_ * LDAO * 2);
        u32x4 sv[14];
        auto pk = [&](const f32x4& a, const f32x4& b) { const f32x4 z = {0.f, 0.f, 0.f, 0.f}; const f32x4 u = __builtin_elementwise_max(a, z), v = __builtin_elementwise_max(b, z);
            unsigned x0 = cvtpk(u[0], u[1]), x1 = cvtpk(u[2], u[3]), y0 = cvtpk(v[0], v[1]), y1 = cvtpk(v[2], v[3]);
            auto r0 = __builtin_amdgcn_permlane16_swap(x0, y0, false, false); auto r1 = __builtin_amdgcn_permlane16_swap(x1, y1, false, false); return (u32x4){r0[0], r1[0], r0[1], r1[1]}; };
        auto off = [&](int s, int half) -> unsigned {
            const int c0 = n0 + 112 * half;
            if (s < 12) return (unsigned)((mrow + 16 * (s & 3)) * LDAO + c0 + 16 * half + 32 * (s >> 2) + 16 * (g & 1) + 8 * (g >> 1)) * 2u;
            return (unsigned)((mrow + 16 * (2 * (s - 12) + (g & 1))) * LDAO + c0 + (half ? 0 : 96) + 8 * (g >> 1)) * 2u; };
        if (wn == 0) {
#pragma unroll
            for (int b = 0; b < 3; ++b)
#pragma unroll
                for (int i = 0; i < 4; ++i) sv[4 * b + i] = pk(acc[i][2 * b], acc[i][2 * b + 1]);
#pragma unroll
            for (int i = 0; i < 2; ++i) sv[12 + i] = pk(acc[2 * i][6], acc[2 * i + 1][6]);
#pragma unroll
            for (int s = 0; s < 14; ++s) *(LAS u32x4*)(lds + HSTG + (wm * 14 + s) * 1024 + lane * 16) = sv[s];
            LGKM_BAR();
        } else {
#pragma unroll
            for (int b = 0; b < 3; ++b)
#pragma unroll
                for (int i = 0; i < 4; ++i) sv[4 * b + i] = pk(acc[i][2 * b + 1], acc[i][2 * b + 2]);
#pragma unroll
            for (int i = 0; i < 2; ++i) sv[12 + i] = pk(acc[2 * i][0], acc[2 * i + 1][0]);
#pragma unroll
            for (int s = 0; s < 14; ++s) st_wt(sv[s], rsa, off(s, 1));
            LGKM_BAR();
#pragma unroll
            for (int s = 0; s < 14; ++s) sv[s] = *(const LAS u32x4*)(lds + HSTG + (wm * 14 + s) * 1024 + lane * 16);
#pragma unroll
            for (int s = 0; s < 14; ++s) st_wt(sv[s], rsa, off(s, 0));
        }
    }
    return T;
}

struct G2 { const bf16_t *ao_, *w2_, *wo_; unsigned aoff, boff[2]; int rb, cb, kh, k0, nkt;
    __device__ __forceinline__ rsrc_t rA() const { return mkrsrc(ao_ + (size_t)rb * 128 * LDAO + k0 * 64, (unsigned)(128 * LDAO - k0 * 64) * 2u); }
    __device__ __forceinline__ rsrc_t rW2() const { return mkrsrc(w2_ + cb * 128, (unsigned)(DFF * D_ - cb * 128) * 2u); }
    __device__ __forceinline__ rsrc_t rWo() const { return mkrsrc(wo_ + cb * 128, (unsigned)(D_ * D_ - cb * 128) * 2u); } };
__device__ __forceinline__ G2 g2_setup(const bf16_t* ao, const bf16_t* w2, const bf16_t* wo, int rb, int cb, int kh, int split) {
    const int lane = threadIdx.x & 63, wid = __builtin_amdgcn_readfirstlane(threadIdx.x >> 6);
    G2 s; s.kh = kh; s.k0 = kh ? split : 0; s.nkt = kh ? 72 - split : split; s.rb = rb; s.cb = cb; s.ao_ = ao; s.w2_ = w2; s.wo_ = wo;
    s.aoff = (unsigned)((16 * wid + (lane >> 3)) * LDAO + (((lane & 7) ^ ((lane >> 3) & 7)) * 8)) * 2u;
#pragma unroll
    for (int i = 0; i < 2; ++i) { const int k = 8 * wid + 4 * i + (lane >> 4), cp = lane & 15, swz = 2 * ((k & 3) | (((k >> 3) & 1) << 2)); s.boff[i] = (unsigned)(k * D_ + (cp ^ swz) * 8) * 2u; }
    return s;
}
__device__ __forceinline__ int g2_slot(int t) { return (int)((0x10002030u >> (8 * (t & 3))) & 0xffu) * 2048; }
__device__ __forceinline__ void g2_stage(lds_ptr lds, const G2& s, int t, int sl) {
    const int wid = __builtin_amdgcn_readfirstlane(threadIdx.x >> 6), gt = s.k0 + t; const lds_ptr d = lds + g2_slot(sl);
#pragma unroll
    for (int i = 0; i < 2; ++i) bl_lds_dev(s.rA(), d + (16 * wid + 8 * i) * 128, s.aoff, t * 128 + i * (8 * LDAO * 2));
    if (gt < 56) {
#pragma unroll
        for (int i = 0; i < 2; ++i) bl_lds(s.rW2(), d + 16384 + (8 * wid + 4 * i) * 256, s.boff[i], gt * (64 * D_ * 2));
    } else {
#pragma unroll
        for (int i = 0; i < 2; ++i) bl_lds(s.rWo(), d + 16384 + (8 * wid + 4 * i) * 256, s.boff[i], (gt - 56) * (64 * D_ * 2));
    }
}

constexpr int AT_KT = 128, AT_STAGE = 4 * AT_KT * 64, AT_NST = 1024 / AT_KT, AT_Q = 122880, AT_FL = 139264;
__device__ __forceinline__ void attn_body(lds_ptr lds, const bf16_t* qkv, bf16_t* ao, unsigned* flags, const unsigned T, const int head, const int qb, const int hpub, const int hwait, const G2& g2) {
    const int tid = threadIdx.x, lane = tid & 63, wid = __builtin_amdgcn_readfirstlane(tid >> 6), wq = wid & 3, kh = wid >> 2;
    const int r32 = lane & 31, hi = lane >> 5;
    const rsrc_t rs = mkrsrc(qkv, 3 * S_ * D_ * 2);
    const unsigned ssrc = (unsigned)((((1 + (wid & 1)) * H_ + head) * S_ + (wid >> 1) * 1024) * 64);
    auto stage = [&](int st, int slot) {
#pragma unroll
        for (int i = 0; i < 8; ++i) bl_lds_dev(rs, lds + slot * AT_STAGE + (wid & 3) * 8192 + i * 1024, lane * 16, ssrc + st * (AT_KT * 64) + i * 1024); };
    if (wid < 4) {
        poll_tags(flags, F_Q + (((lane >> 3) & 3) * 1024 + head * 32) / 192 * 32 + (lane & 7), T, 24);
#pragma unroll
        for (int i = 0; i < 4; ++i) bl_lds_dev(rs, lds + AT_Q + wid * 4096 + i * 1024, lane * 16, (unsigned)((head * S_ + qb * 256) * 64 + wid * 4096 + i * 1024));
    }
    const int kr[2] = {r32 * 64 + ((hi ^ ((r32 >> 2) & 3)) * 16), r32 * 64 + (((2 + hi) ^ ((r32 >> 2) & 3)) * 16)};
    const int vr = AT_KT * 64 + (4 * hi + ((lane & 15) >> 2)) * 64 + ((lane >> 4) & 1) * 32 + (lane & 3) * 8;
    bf16x8 sel;
    { const short one = ((lane & 15) == ((lane >> 4) & 1)) ? (short)0x3f80 : (short)0;
#pragma unroll
      for (int j = 0; j < 8; ++j) sel[j] = one; }
    bf16x8 qf[2][2];
    float mref[2];
    f32x16 o[2]; f32x4 ls[2];
    for (int attempt = 0; attempt < 2; ++attempt) {
        if (wid < 4) { stage(0, 0); stage(1, 1); asm volatile("s_waitcnt vmcnt(8)" ::: "memory"); WAIT_BAR(8); } else LGKM_BAR();
        if (attempt == 0) {
#pragma unroll
            for (int a = 0; a < 2; ++a)
#pragma unroll
                for (int s = 0; s < 2; ++s) qf[a][s] = lds_b128(lds + AT_Q + (wq * 64 + a * 32 + r32) * 64 + (2 * s + hi) * 16);
            const bf16x8 k0 = lds_b128(lds + kr[0]), k1 = lds_b128(lds + kr[1]);
#pragma unroll
            for (int a = 0; a < 2; ++a) { f32x16 s = {}; s = MFMA32(k0, qf[a][0], s); s = MFMA32(k1, qf[a][1], s);
                float m = s[0];
#pragma unroll
                for (int r = 1; r < 16; ++r) m = fmaxf(m, s[r]);
                auto sw = __builtin_amdgcn_permlane32_swap(__float_as_uint(m), __float_as_uint(m), false, false);
                mref[a] = fmaxf(__uint_as_float(sw[0]), __uint_as_float(sw[1])); }
        }
        f32x16 nm[2];
#pragma unroll
        for (int a = 0; a < 2; ++a) {
#pragma unroll
            for (int r = 0; r < 16; ++r) nm[a][r] = -mref[a];
            o[a] = (f32x16){}; ls[a] = (f32x4){}; }
        bf16x8 kA[2], kB[2]; s16x4 vA[4], vB[4]; f32x16 sa_, sb_; u32x4 pa0, pa1, pb0, pb1;
        { const lds_ptr base = lds + kh * (2 * AT_KT * 64);
          kA[0] = lds_b128(base + kr[0]); kA[1] = lds_b128(base + kr[1]);
          sa_ = MFMA32(kA[0], qf[0][0], nm[0]); sa_ = MFMA32(kA[1], qf[0][1], sa_);
          pb0 = (u32x4){0u, 0u, 0u, 0u}; pb1 = pb0;
#pragma unroll
          for (int i = 0; i < 4; ++i) vB[i] = (s16x4){0, 0, 0, 0}; }
#define EXP2(S, i) S[i] = __builtin_amdgcn_exp2f(S[i])
#define PF(x) __builtin_bit_cast(bf16x8, x)
#define ASTEP(SC, PC0, PC1, SN, KF, QN, NMN, ON, LN_, VF, PP0, PP1, R0, R1, R2, R3, R4, R5) do { \
            SN = MFMA32(KF[0], QN[0], NMN); SB0(); EXP2(SC, 0); EXP2(SC, 1); EXP2(SC, 2); R0; SB0(); \
            SN = MFMA32(KF[1], QN[1], SN); SB0(); EXP2(SC, 3); EXP2(SC, 4); EXP2(SC, 5); PC0[0] = cvtpk(SC[0], SC[1]); R1; SB0(); \
            ON = MFMA32(cat4(VF[0], VF[1]), PF(PP0), ON); SB0(); EXP2(SC, 6); EXP2(SC, 7); EXP2(SC, 8); PC0[1] = cvtpk(SC[2], SC[3]); R2; SB0(); \
            ON = MFMA32(cat4(VF[2], VF[3]), PF(PP1), ON); SB0(); EXP2(SC, 9); EXP2(SC, 10); EXP2(SC, 11); PC0[2] = cvtpk(SC[4], SC[5]); R3; SB0(); \
            LN_ = MFMA16(sel, PF(PP0), LN_); SB0(); EXP2(SC, 12); EXP2(SC, 13); PC0[3] = cvtpk(SC[6], SC[7]); PC1[0] = cvtpk(SC[8], SC[9]); R4; SB0(); \
            LN_ = MFMA16(sel, PF(PP1), LN_); SB0(); EXP2(SC, 14); EXP2(SC, 15); PC1[1] = cvtpk(SC[10], SC[11]); PC1[2] = cvtpk(SC[12], SC[13]); R5; PC1[3] = cvtpk(SC[14], SC[15]); asm volatile("" : "+v"(PC0), "+v"(PC1)); SB0(); } while (0)
#define ATILE(KC, VP, KN, VN, kb, vb) do { \
            ASTEP(sa_, pa0, pa1, sb_, KC, qf[1], nm[1], o[1], ls[1], VP, pb0, pb1, KN[0] = lds_b128((kb) + kr[0]), KN[1] = lds_b128((kb) + kr[1]), \
                  VN[0] = lds_tr((vb) + vr), VN[1] = lds_tr((vb) + vr + 512), VN[2] = lds_tr((vb) + vr + 1024), VN[3] = lds_tr((vb) + vr + 1536)); \
            ASTEP(sb_, pb0, pb1, sa_, KN, qf[0], nm[0], o[0], ls[0], VN, pa0, pa1, (void)0, (void)0, (void)0, (void)0, (void)0, (void)0); } while (0)
        const unsigned aev = wid >= 4 ? 0x54u : (0xc0u | (wid == 0 ? 0x24u : 0u) | ((wid == 1 && blockIdx.x == 0) ? 0x10u : 0u));
        int slot = 0;
        for (int st = 0; st < AT_NST; ++st) {
            const int nslot = (slot == 2) ? 0 : slot + 1;
            const lds_ptr base = lds + slot * AT_STAGE + kh * (2 * AT_KT * 64), nbase = lds + nslot * AT_STAGE + kh * (2 * AT_KT * 64);
            SB0();
            ATILE(kA, vB, kB, vA, base + 2048, base);
            ATILE(kB, vA, kA, vB, base + 4096, base + 2048);
            ATILE(kA, vB, kB, vA, base + 6144, base + 4096);
            if (__builtin_expect((aev >> st) & 1u, 0)) {
                if (wid < 4) {
                    if (attempt == 0 && st == 5 && wid == 0) poll_tags(flags, hwait + lane, T, 16);
                    if (attempt == 0 && st == 4) {
#pragma unroll 1
                        for (int i = 0; i < 4; ++i) poll_tags(flags, F_ST + 64 * i + lane, T, 64);
                        if (lane == 0) __hip_atomic_store(flags + F_E, T, RLX_AGENT); }
                    WAIT_BAR(0);
                    if (st + 2 < AT_NST) stage(st + 2, (slot == 0) ? 2 : slot - 1);
                } else {
                    WAIT_BAR(0);
                    if (attempt == 0 && st < 6) {
                        const int w = (blockIdx.x >> 3) * 4 + wid - 4; const bool second = st == 4;
                        const int n = g2.kh ? (second ? 8 : 10) : 9, k0 = g2.kh ? (second ? w * 8 : 2304 + w * 10) : w * 18 + (second ? 9 : 0);
                        const rsrc_t rp = mkrsrc(((g2.kh && second) ? g2.wo_ : g2.w2_) + (g2.cb >> 2) * 512, (DFF * D_ - 512) * 2);
                        for (int i = 0; i < n; ++i) bl_lds(rp, lds + JUNK, lane * 16, (k0 + i) * (D_ * 2)); } }
                if (attempt == 0 && st == 2 && hpub >= 0) publish(flags, hpub, T);
                if (attempt == 0 && st == AT_NST - 2) g2_stage(lds, g2, 0, 0);
            } else if (wid < 4) { WAIT_BAR(0); stage(st + 2, (slot == 0) ? 2 : slot - 1); }
            else LGKM_BAR();
            SB0();
            ATILE(kB, vA, kA, vB, nbase, base + 6144);
            slot = nslot;
        }
        o[1] = MFMA32(cat4(vB[0], vB[1]), PF(pb0), o[1]); o[1] = MFMA32(cat4(vB[2], vB[3]), PF(pb1), o[1]);
        ls[1] = MFMA16(sel, PF(pb0), ls[1]); ls[1] = MFMA16(sel, PF(pb1), ls[1]);
#undef ATILE
#undef ASTEP
#undef EXP2
#undef PF
        LGKM_BAR();
        if (attempt == 1) break;
        bool bad = false;
#pragma unroll
        for (int a = 0; a < 2; ++a) { bad |= !(ls[a][0] < 1.0e18f) || !(ls[a][1] < 1.0e18f);
#pragma unroll
            for (int r = 0; r < 16; ++r) bad |= !(fabsf(o[a][r]) < 1.0e30f); }
        LAS unsigned* fl = (LAS unsigned*)(lds + AT_FL);
        if (lane == 0) fl[wid] = __any(bad) ? 1u : 0u;
        LGKM_BAR();
        const u32x4 f0 = *(const LAS u32x4*)fl, f1 = *(const LAS u32x4*)(fl + 4);
        if ((f0[0] | f0[1] | f0[2] | f0[3] | f1[0] | f1[1] | f1[2] | f1[3]) == 0u) break;
        float mx[2] = {-INFINITY, -INFINITY};
        for (int st = 0; st < AT_NST; ++st) {
            if (wid < 4) stage(st, 0);
            WAIT_BAR(0);
#pragma unroll
            for (int h2 = 0; h2 < 2; ++h2)
                for (int kt = 0; kt < AT_KT / 32; ++kt) {
                    const lds_ptr base = lds + h2 * (2 * AT_KT * 64) + kt * 2048;
                    const bf16x8 k0 = lds_b128(base + kr[0]), k1 = lds_b128(base + kr[1]);
#pragma unroll
                    for (int a = 0; a < 2; ++a) { f32x16 s = {}; s = MFMA32(k0, qf[a][0], s); s = MFMA32(k1, qf[a][1], s);
#pragma unroll
                        for (int r = 0; r < 16; ++r) mx[a] = fmaxf(mx[a], s[r]); }
                }
            LGKM_BAR();
        }
#pragma unroll
        for (int a = 0; a < 2; ++a) { auto sw = __builtin_amdgcn_permlane32_swap(__float_as_uint(mx[a]), __float_as_uint(mx[a]), false, false);
            mref[a] = fmaxf(__uint_as_float(sw[0]), __uint_as_float(sw[1])); }
    }
    g2_stage(lds, g2, 1, 1);
    LAS float* xo = (LAS float*)lds + wq * (2 * 17 * 64);
    if (kh == 1) {
#pragma unroll
        for (int a = 0; a < 2; ++a) {
#pragma unroll
            for (int r = 0; r < 16; ++r) xo[(a * 17 + r) * 64 + lane] = o[a][r];
            if (lane < 16) { xo[(a * 17 + 16) * 64 + lane] = ls[a][0]; xo[(a * 17 + 16) * 64 + 16 + lane] = ls[a][1]; } }
    } else {
#pragma unroll
        for (int a = 0; a < 2; ++a) if (lane < 16) { xo[(a * 17 + 16) * 64 + 32 + lane] = ls[a][0]; xo[(a * 17 + 16) * 64 + 48 + lane] = ls[a][1]; }
    }
    LGKM_BAR();
    if (kh == 0) {
        const rsrc_t rsa = mkrsrc(ao, S_ * LDAO * 2);
#pragma unroll
        for (int a = 0; a < 2; ++a) {
            const float l = xo[(a * 17 + 16) * 64 + r32] + xo[(a * 17 + 16) * 64 + 32 + r32];
            const float rl = 1.0f / l;
            unsigned pk[8];
#pragma unroll
            for (int r = 0; r < 8; ++r) pk[r] = cvtpk((o[a][2 * r] + xo[(a * 17 + 2 * r) * 64 + lane]) * rl, (o[a][2 * r + 1] + xo[(a * 17 + 2 * r + 1) * 64 + lane]) * rl);
            const int dst = (qb * 256 + wq * 64 + a * 32 + r32) * LDAO + DFF + head * DH;
#pragma unroll
            for (int k = 0; k < 4; k += 2) {
                auto r0 = __builtin_amdgcn_permlane32_swap(pk[2 * k], pk[2 * k + 2], false, false); auto r1 = __builtin_amdgcn_permlane32_swap(pk[2 * k + 1], pk[2 * k + 3], false, false);
                st_wt((u32x4){r0[0], r1[0], r0[1], r1[1]}, rsa, (unsigned)(dst + 8 * k + 8 * hi) * 2u);
            }
        }
    }
    LGKM_BAR();
}

__device__ __forceinline__ void gemm2_body(lds_ptr lds, const G2& g2, const bf16_t* __restrict__ x, bf16_t* __restrict__ out, float* part, unsigned* flags, const unsigned T, const int rb, const int cb, const int kh, const int opub) {
    const int NKT = g2.nkt;
    const int tid = threadIdx.x, lane = tid & 63, wid = __builtin_amdgcn_readfirstlane(tid >> 6), kg = wid >> 2, wm = (wid >> 1) & 1, wn = wid & 1;
    const int fr = lane & 15, g = lane >> 4, q = fr >> 2, p = lane & 3;
    const int m0 = rb * 128, n0 = cb * 128;
    const int ar = (64 * wm + fr) * 128 + (((4 * kg + g) ^ (fr & 7)) * 16);
    int br[4];
    { const int swz = 2 * (q | ((g & 1) << 2));
#pragma unroll
      for (int j = 0; j < 4; ++j) br[j] = 16384 + (32 * kg + 8 * g + q) * 256 + (((2 * (4 * wn + j) + (p >> 1)) ^ swz) * 16) + (p & 1) * 8; }
    f32x4 acc[4][4];
#pragma unroll
    for (int i = 0; i < 4; ++i)
#pragma unroll
        for (int j = 0; j < 4; ++j) acc[i][j] = (f32x4){0.f, 0.f, 0.f, 0.f};
    bf16x8 A0[4], A1[4]; s16x4 B0[4][2], B1[4][2];
    const int wid4 = wid; const rsrc_t rA2 = g2.rA(), rW2 = g2.rW2(), rWo = g2.rWo();
    auto st1 = [&](int t, int sl, int i) {
        const int gt = g2.k0 + t; const lds_ptr d = lds + g2_slot(sl);
        if (i < 2) dma16<true, 0>(rA2, d + (16 * wid4 + 8 * i) * 128, g2.aoff, t * 128 + i * (8 * LDAO * 2));
        else { const bool w = gt < 56; dma16<false, 0>(w ? rW2 : rWo, d + 16384 + (8 * wid4 + 4 * (i - 2)) * 256, g2.boff[i - 2], (w ? gt : gt - 56) * (64 * D_ * 2)); } };
#define RDONE(AN, BN_, so, r) do { if ((r) < 4) AN[(r) & 3] = lds_b128(lds + (so) + ar + ((r) & 3) * 2048); \
        else if ((r) < 12) BN_[(((r) - 4) >> 1) & 3][(r) & 1] = lds_tr(lds + (so) + br[(((r) - 4) >> 1) & 3] + ((r) & 1) * 1024); } while (0)
#define STEP(AC, BC, AN, BN_, so, tl, sl) do { \
        unroll<0, 16>([&](auto ic) { constexpr int idx = decltype(ic)::value; constexpr int j = idx >> 2, i = idx & 3; \
            acc[i][j] = MFMA16(cat4(BC[j][0], BC[j][1]), AC[i], acc[i][j]); SB0(); \
            RDONE(AN, BN_, so, idx); \
            if ((idx & 3) == 2) st1(tl, sl, idx >> 2); \
            SB0(); }); } while (0)
    g2_stage(lds, g2, 2, 2); g2_stage(lds, g2, 3, 3);
    WAIT_BAR(8);
    publish(flags, opub, T);
    unroll<0, 12>([&](auto ic) { constexpr int idx = decltype(ic)::value; RDONE(A0, B0, g2_slot(0), idx); });
    WAIT_BAR(8);
    for (int t = 0; t < NKT; t += 2) {
        if (kh == 1 && t == 48 - g2.k0 && wid == 0) poll_tags(flags, F_O + (rb >> 1) * 32 + (lane & 31), T, 32);
        SB0();
        STEP(A0, B0, A1, B1, g2_slot(t + 1), min(t + 4, NKT - 1), t);
        WAIT_BAR(8); SB0();
        STEP(A1, B1, A0, B0, g2_slot(t + 2), min(t + 5, NKT - 1), t + 1);
        WAIT_BAR(8);
    }
#undef RDONE
#undef STEP
    WAIT_BAR(0);
    const int mrow = m0 + 64 * wm + 32 * kg + 16 * kh + fr;
    u32x2 xv[4];
#pragma unroll
    for (int j = 0; j < 4; ++j) xv[j] = *(const u32x2*)(x + (size_t)mrow * D_ + n0 + 64 * wn + 16 * j + 4 * g);
    LAS f32x4* xr = (LAS f32x4*)lds + (wid & 3) * 1024;
#pragma unroll
    for (int i = 0; i < 2; ++i)
#pragma unroll
        for (int j = 0; j < 4; ++j) xr[(kg * 8 + i * 4 + j) * 64 + lane] = kg ? acc[i][j] : acc[2 + i][j];
    LGKM_BAR();
    f32x4 r[2][4];
#pragma unroll
    for (int i = 0; i < 2; ++i)
#pragma unroll
        for (int j = 0; j < 4; ++j) r[i][j] = (kg ? acc[2 + i][j] : acc[i][j]) + xr[((kg ^ 1) * 8 + i * 4 + j) * 64 + lane];
    const int tile = rb * 8 + cb;
    { const rsrc_t rs = mkrsrc(part + (size_t)(tile * 2 + kh) * 8192, 32768);
#pragma unroll
      for (int j = 0; j < 4; ++j) st_wt(__builtin_bit_cast(u32x4, kh ? r[0][j] : r[1][j]), rs, (wid * 4 + j) * 1024 + lane * 16); }
    asm volatile("s_waitcnt vmcnt(0)" ::: "memory");
    if (lane == 0) __hip_atomic_store(flags + F_P + (tile * 2 + kh) * 32 + wid, T, RLX_AGENT);
    poll_tags(flags, F_P + (tile * 2 + (kh ^ 1)) * 32 + wid, T, 1);
    { const rsrc_t rs = mkrsrc(part + (size_t)(tile * 2 + (kh ^ 1)) * 8192, 32768);
      f32x4 o[4];
#pragma unroll
      for (int j = 0; j < 4; ++j) o[j] = __builtin_bit_cast(f32x4, __builtin_amdgcn_raw_buffer_load_b128(rs, (wid * 4 + j) * 1024 + lane * 16, 0, 16));
      unsigned pk[4][2];
#pragma unroll
      for (int j = 0; j < 4; ++j) { f32x4 v = (kh ? r[1][j] : r[0][j]) + o[j];
          v[0] += __uint_as_float(xv[j][0] << 16); v[1] += __uint_as_float(xv[j][0] & 0xffff0000u); v[2] += __uint_as_float(xv[j][1] << 16); v[3] += __uint_as_float(xv[j][1] & 0xffff0000u);
          pk[j][0] = cvtpk(v[0], v[1]); pk[j][1] = cvtpk(v[2], v[3]); }
#pragma unroll
      for (int j = 0; j < 4; j += 2) {
          auto r0 = __builtin_amdgcn_permlane16_swap(pk[j][0], pk[j + 1][0], false, false); auto r1 = __builtin_amdgcn_permlane16_swap(pk[j][1], pk[j + 1][1], false, false);
          *(u32x4*)(out + (size_t)mrow * D_ + n0 + 64 * wn + 16 * (j + (g & 1)) + 8 * (g >> 1)) = (u32x4){r0[0], r1[0], r0[1], r1[1]}; } }
}

__global__ __launch_bounds__(512, 2) void fused_kernel(const bf16_t* __restrict__ wqkv, const bf16_t* __restrict__ w1, const bf16_t* __restrict__ w2, const bf16_t* __restrict__ wo,
                                                       const bf16_t* __restrict__ x, bf16_t* qkv, bf16_t* ao, bf16_t* __restrict__ out, float* part, unsigned* flags) {
    __shared__ __attribute__((aligned(16))) char smem[LDS_BYTES];
    const lds_ptr lds = (lds_ptr)smem;
    const int xg = blockIdx.x & 7, r = blockIdx.x >> 3;
    {
        unsigned long long pc; unsigned len;
        asm volatile("s_getpc_b64 %0\n.Lfk_beg:\n\ts_mov_b32 %1, .Lfk_end-.Lfk_beg" : "=s"(pc), "=s"(len));
        if (threadIdx.x >= 448) { const rsrc_t rc = mkrsrc((const void*)pc, len);
            bl_lds(rc, lds + JUNK, (threadIdx.x & 63) * 16, r * 2048); bl_lds(rc, lds + JUNK, (threadIdx.x & 63) * 16, r * 2048 + 1024); }
    }
    const int rb1 = (xg & 1) * 4 + (r & 3), c1 = (xg >> 1) * 4 + ((r >> 2) & 3), ffn = r >> 4;
    unsigned T;
    if (ffn) T = gemm1_body<7>(lds, x, w1, DFF, rb1 * 256, c1 * 224, qkv, ao, flags, 16 + c1, wqkv, w1);
    else { T = gemm1_body<6>(lds, x, wqkv, 3 * D_, rb1 * 256, c1 * 192, qkv, ao, flags, c1, wqkv, w1); publish(flags, F_Q + c1 * 32 + rb1, T); }
    const int kh = xg & 1, rr = kh ? (r ^ 16) : r, rb3 = ((xg >> 1) & 1) * 8 + (rr & 7), cb3 = (xg >> 2) * 4 + (rr >> 3);
    const G2 g2 = g2_setup(ao, w2, wo, rb3, cb3, kh, (rr >> 4) ? 72 - SPLITQ : SPLITQ);
    const int head = xg * 4 + (r >> 3), qb = r & 7;
    attn_body(lds, qkv, ao, flags, T, head, qb, ffn ? F_H + rb1 * 32 + c1 : -1, F_H + (rb3 >> 1) * 32, g2);
    gemm2_body(lds, g2, x, out, part, flags, T, rb3, cb3, kh, F_O + qb * 32 + head);
    asm volatile(".Lfk_end:");
}

extern "C" void kernel_launch(void* const* d_in, const int* in_sizes, int n_in, void* d_out, int out_size, void* d_ws, size_t ws_size, hipStream_t stream) {
    const bf16_t* x = (const bf16_t*)d_in[0]; const bf16_t* wqkv = (const bf16_t*)d_in[1]; const bf16_t* wo = (const bf16_t*)d_in[2];
    const bf16_t* w1 = (const bf16_t*)d_in[4]; const bf16_t* w2 = (const bf16_t*)d_in[5];
    char* ws = (char*)d_ws;
    unsigned* flags = (unsigned*)(ws + WS_FLAGS); bf16_t* qkv = (bf16_t*)(ws + WS_QKV); bf16_t* ao = (bf16_t*)(ws + WS_AO); float* part = (float*)(ws + WS_PART);
    fused_kernel<<<256, 512, 0, stream>>>(wqkv, w1, w2, wo, x, qkv, ao, (bf16_t*)d_out, part, flags);
}
```

```cpp
#include <hip/hip_runtime.h>
#include <hip/hip_bf16.h>
#include <stdint.h>

typedef unsigned short bf16_t;
typedef short bf16x8 __attribute__((ext_vector_type(8)));
typedef short s16x4 __attribute__((ext_vector_type(4)));
typedef float f32x2 __attribute__((ext_vector_type(2)));
typedef float f32x4 __attribute__((ext_vector_type(4)));
typedef float f32x16 __attribute__((ext_vector_type(16)));
typedef unsigned u32x4 __attribute__((ext_vector_type(4)));
typedef unsigned u32x2 __attribute__((ext_vector_type(2)));
typedef __bf16 bf16x2n __attribute__((ext_vector_type(2)));
#define LAS __attribute__((address_space(3)))
typedef LAS char* lds_ptr;

constexpr int S_ = 2048, D_ = 1024, H_ = 32, DH = 32, DFF = 3584;
constexpr int LDAO = DFF + D_;
constexpr size_t WS_FLAGS = 0;
constexpr size_t WS_QKV = 1u << 20;
constexpr size_t WS_AO = WS_QKV + (size_t)3 * S_ * D_ * 2;
constexpr size_t WS_PART = WS_AO + (size_t)S_ * LDAO * 2;
constexpr int LDS_BYTES = 163840;
constexpr int F_E = 0, F_ST = 256, F_Q = 1024, F_H = 2048, F_O = 3072, F_P = 4096;
constexpr int F_LT = 512;
constexpr int F_LN = 16384, F_CS = 32768;
constexpr int NCOL = 3 * D_ + DFF;
constexpr int SPLITQ = 40;
constexpr int TBL = 151552;


__device__ __forceinline__ unsigned cvtpk(float lo, float hi) { return __builtin_bit_cast(unsigned, __builtin_convertvector((f32x2){lo, hi}, bf16x2n)); }
__device__ __forceinline__ float rbf(float x) { return __uint_as_float(cvtpk(x, x) << 16); }
typedef __amdgpu_buffer_rsrc_t rsrc_t;
__device__ __forceinline__ rsrc_t mkrsrc(const void* p, unsigned bytes) { return __builtin_amdgcn_make_buffer_rsrc((void*)p, 0, bytes, 0x00020000); }
template <bool DEV, int NOP> __device__ __forceinline__ void dma16(rsrc_t r, lds_ptr l, unsigned voff, unsigned soff) {
    const unsigned a = __builtin_amdgcn_readfirstlane((unsigned)(uintptr_t)l), so = __builtin_amdgcn_readfirstlane(soff);
    if (DEV) asm volatile("s_mov_b32 m0, %0\n\ts_nop %4\n\tbuffer_load_dwordx4 %1, %2, %3 offen sc1 lds" :: "s"(a), "v"(voff), "s"(r), "s"(so), "n"(NOP) : "memory");
    else asm volatile("s_mov_b32 m0, %0\n\ts_nop %4\n\tbuffer_load_dwordx4 %1, %2, %3 offen lds" :: "s"(a), "v"(voff), "s"(r), "s"(so), "n"(NOP) : "memory"); }
__device__ __forceinline__ void bl_lds(rsrc_t r, lds_ptr l, unsigned voff, unsigned soff) { dma16<false, 3>(r, l, voff, soff); }
__device__ __forceinline__ void bl_lds_dev(rsrc_t r, lds_ptr l, unsigned voff, unsigned soff) { dma16<true, 3>(r, l, voff, soff); }
__device__ __forceinline__ void st_wt(u32x4 v, rsrc_t r, unsigned off) { __builtin_amdgcn_raw_buffer_store_b128(v, r, off, 0, 16); }
__device__ __forceinline__ bf16x8 lds_b128(lds_ptr p) { return *(const LAS bf16x8*)p; }
__device__ __forceinline__ s16x4 lds_tr(lds_ptr p) { return __builtin_amdgcn_ds_read_tr16_b64_v4i16((LAS s16x4*)p); }
__device__ __forceinline__ bf16x8 cat4(s16x4 a, s16x4 b) { return (bf16x8){a[0], a[1], a[2], a[3], b[0], b[1], b[2], b[3]}; }
#define MFMA16(a, b, c) __builtin_amdgcn_mfma_f32_16x16x32_bf16(a, b, c, 0, 0, 0)
#define MFMA32(a, b, c) __builtin_amdgcn_mfma_f32_32x32x16_bf16(a, b, c, 0, 0, 0)
#define WAIT_BAR(N) asm volatile("s_waitcnt vmcnt(%0) lgkmcnt(0)\n\ts_barrier" :: "n"(N) : "memory")
#define LGKM_BAR() asm volatile("s_waitcnt lgkmcnt(0)\n\ts_barrier" ::: "memory")
#define SB0() __builtin_amdgcn_sched_barrier(0)
#define RLX_AGENT __ATOMIC_RELAXED, __HIP_MEMORY_SCOPE_AGENT
template <int I> struct IC { static constexpr int value = I; };
template <int B, int E, class F> __device__ __forceinline__ void unroll(F&& f) { if constexpr (B < E) { f(IC<B>{}); unroll<B + 1, E>(f); } }

__device__ __forceinline__ float wave_sum(float v) {
    v += __builtin_bit_cast(float, __builtin_amdgcn_update_dpp(0, __builtin_bit_cast(int, v), 0xB1, 0xf, 0xf, true));
    v += __builtin_bit_cast(float, __builtin_amdgcn_update_dpp(0, __builtin_bit_cast(int, v), 0x4E, 0xf, 0xf, true));
    v += __builtin_bit_cast(float, __builtin_amdgcn_update_dpp(0, __builtin_bit_cast(int, v), 0x141, 0xf, 0xf, true));
    v += __builtin_bit_cast(float, __builtin_amdgcn_update_dpp(0, __builtin_bit_cast(int, v), 0x140, 0xf, 0xf, true));
    { auto r = __builtin_amdgcn_permlane16_swap(__float_as_uint(v), __float_as_uint(v), false, false); v = __uint_as_float(r[0]) + __uint_as_float(r[1]); }
    { auto r = __builtin_amdgcn_permlane32_swap(__float_as_uint(v), __float_as_uint(v), false, false); v = __uint_as_float(r[0]) + __uint_as_float(r[1]); }
    return v;
}
__device__ __forceinline__ void poll_tags(unsigned* flags, int word, unsigned T, int n) {
    const bool act = (int)(threadIdx.x & 63) < n; unsigned spins = 0;
    for (;;) { unsigned v = T; if (act) v = __hip_atomic_load(flags + word, RLX_AGENT);
        if (__all(v == T)) break; __builtin_amdgcn_s_sleep(2); if (++spins > (1u << 22)) break; }
    asm volatile("" ::: "memory");
}
__device__ __forceinline__ void publish(unsigned* flags, int word, unsigned T) {
    if (threadIdx.x == 0) __hip_atomic_store(flags + word, T, RLX_AGENT);
}

constexpr int JUNK = 162816;
constexpr int HSTG = 65536;
template <int NT>
__device__ __forceinline__ unsigned gemm1_body(lds_ptr lds, const bf16_t* xn, const bf16_t* W, const int ldw, const int m0, const int n0, bf16_t* qkv, bf16_t* ao, unsigned* flags, const int j32, const bf16_t* wq_, const bf16_t* w1_) {
    constexpr int BN = 32 * NT, RB = BN * 2, CPR = BN / 8, BSLOT = 64 * RB, NKT = D_ / 64, GB = (NT == 7) ? 4 : 3;
    constexpr int LB = 65536, NMF = 4 * NT, NRD = 4 + 2 * NT, NBB = (NT == 7) ? 2 : 4;
    const int tid = threadIdx.x, lane = tid & 63, wid = __builtin_amdgcn_readfirstlane(tid >> 6), wm = wid & 3, wn = wid >> 2;
    const int fr = lane & 15, g = lane >> 4, q = fr >> 2, p = lane & 3;
    const rsrc_t rA = mkrsrc(xn + (size_t)m0 * D_, 256 * D_ * 2), rB = mkrsrc(W + n0, (unsigned)(D_ * ldw - n0) * 2u);
    const unsigned aoff = (unsigned)((32 * wid + (lane >> 3)) * D_ + (((lane & 7) ^ ((lane >> 3) & 7)) * 8)) * 2u;
    unsigned boff[GB];
#pragma unroll
    for (int i = 0; i < GB; ++i) { const int idx = 64 * i + lane, rl = idx / CPR, cp = idx % CPR, k = (8 * wid + rl) & 63;
        const int swz = (NT == 7) ? 2 * ((k >> 3) & 1) : 2 * (((k >> 1) & 1) | (((k >> 3) & 1) << 1));
        boff[i] = (unsigned)(k * ldw + (cp ^ swz) * 8) * 2u; }
    const int krot = (m0 >> 10) * 8, tcs = ((m0 >> 8) & 3) * 2;
    auto stageA1 = [&](int kt_, int slot, int i) { const int kt = (kt_ + krot) & (NKT - 1); dma16<false, 0>(rA, lds + slot * 32768 + (32 * wid + 8 * i) * 128, aoff, kt * 128 + i * (8 * D_ * 2)); };
    auto stageB1 = [&](int kt_, int slot, int i) { const int kt = (kt_ + krot) & (NKT - 1); const unsigned so = (unsigned)(kt * 64 * ldw) * 2u;
        if (NT == 7 && i == 3) { if (lane < 32) dma16<false, 0>(rB, lds + LB + slot * BSLOT + (8 * wid * CPR + 192) * 16, boff[GB - 1], so); }
        else dma16<false, 0>(rB, lds + LB + slot * BSLOT + (8 * wid * CPR + 64 * i) * 16, boff[i < GB ? i : 0], so); };
    int ar[2];
#pragma unroll
    for (int ks = 0; ks < 2; ++ks) ar[ks] = (64 * wm + fr) * 128 + (((4 * ks + g) ^ (fr & 7)) * 16);
    int br[NBB];
    { const int swz = (NT == 7) ? 2 * (g & 1) : 2 * ((q >> 1) | ((g & 1) << 1));
#pragma unroll
      for (int j = 0; j < NBB; ++j) br[j] = LB + (8 * g + q) * RB + (((2 * (NT * wn + j) + (p >> 1)) ^ swz) * 16) + (p & 1) * 8; }
    f32x4 acc[4][NT];
#pragma unroll
    for (int i = 0; i < 4; ++i)
#pragma unroll
        for (int j = 0; j < NT; ++j) acc[i][j] = (f32x4){0.f, 0.f, 0.f, 0.f};
    bf16x8 A0[4], A1[4]; s16x4 B0[NT][2], B1[NT][2];
#define RDONE(AN, BN_, sa, sb, ks, r) do { if ((r) < 4) AN[(r) < 4 ? (r) : 0] = lds_b128(lds + (sa) + ar[ks] + ((r) < 4 ? (r) : 0) * 2048); \
        else if ((r) < NRD) { constexpr int jj = (((r) - 4) >> 1) < NT && (r) >= 4 ? (((r) - 4) >> 1) : 0, tt = ((r) - 4) & 1; BN_[jj][tt] = lds_tr(lds + (sb) + br[jj % NBB] + 32 * (jj - jj % NBB) + (32 * (ks) + 4 * tt) * RB); } } while (0)
#define SUBSTEP(AC, BC, AN, BN_, sa, sb, ks, STG, go, tl, sl) do { \
        unroll<0, NMF>([&](auto ic) { constexpr int idx = decltype(ic)::value; constexpr int j = idx >> 2, i = idx & 3; \
            acc[i][j] = MFMA16(cat4(BC[j][0], BC[j][1]), AC[i], acc[i][j]); SB0(); \
            RDONE(AN, BN_, sa, sb, ks, idx); \
            if ((idx % NT) == 1 && (go)) { if (STG == 1) stageA1(tl, sl, idx / NT); if (STG == 2 && idx / NT < GB) stageB1(tl, sl, idx / NT); } \
            SB0(); }); } while (0)
#pragma unroll
    for (int t = 0; t < 2; ++t) {
#pragma unroll
        for (int i = 0; i < GB; ++i) stageB1(t, t, i);
#pragma unroll
        for (int i = 0; i < 4; ++i) stageA1(t, t, i); }
    unsigned T; float lnv;
    const int ncg = (NT == 7 ? 3 * D_ : 0) + n0;
    {   const int row = m0 + 8 * j32 + wid;
        const u32x4* src = (const u32x4*)(xn + (size_t)row * D_);
        u32x4 v0, v1; unsigned ev;
        asm volatile("global_load_dwordx4 %0, %3, off\n\tglobal_load_dwordx4 %1, %3, off offset:1024\n\tglobal_load_dword %2, %4, off sc1"
                     : "=&v"(v0), "=&v"(v1), "=&v"(ev) : "v"(src + lane), "v"(flags + F_E) : "memory");
        asm volatile("s_waitcnt vmcnt(0)" : "+v"(v0), "+v"(v1), "+v"(ev) :: "memory");
        SB0();
        float f[16];
#pragma unroll
        for (int i = 0; i < 4; ++i) { f[2 * i] = __uint_as_float(v0[i] << 16); f[2 * i + 1] = __uint_as_float(v0[i] & 0xffff0000u); f[8 + 2 * i] = __uint_as_float(v1[i] << 16); f[8 + 2 * i + 1] = __uint_as_float(v1[i] & 0xffff0000u); }
        float sm = 0.f;
#pragma unroll
        for (int i = 0; i < 16; ++i) sm += f[i];
        const float mean = wave_sum(sm) * (1.0f / D_);
        float qq = 0.f;
#pragma unroll
        for (int i = 0; i < 16; ++i) { const float d = f[i] - mean; qq += d * d; }
        qq = wave_sum(qq);
        T = (unsigned)__builtin_amdgcn_readfirstlane(ev) + 1u;
        const float sd = rbf(__fsqrt_rn(rbf(rbf(qq * (1.0f / D_)) + 1e-5f)));
        lnv = (lane & 1) ? 1.0f / sd : rbf(mean);
    }
    WAIT_BAR(0);
    auto colsum = [&](s16x4 (&BC)[NT][2]) {
        const bf16x8 ones = {0x3f80, 0x3f80, 0x3f80, 0x3f80, 0x3f80, 0x3f80, 0x3f80, 0x3f80}; const f32x4 z = {0.f, 0.f, 0.f, 0.f};
        float c[NT];
#pragma unroll
        for (int j = 0; j < NT; ++j) c[j] = MFMA16(ones, cat4(BC[j][0], BC[j][1]), z)[0];
        if (lane < 16) {
#pragma unroll
            for (int j = 0; j < NT; ++j) ((LAS float*)(lds + TBL + 2048))[wm * 256 + 16 * (NT * wn + j) + lane] = c[j]; } };
    unroll<0, NRD>([&](auto ic) { constexpr int idx = decltype(ic)::value; RDONE(A0, B0, 0, 0, 0, idx); });
    int sa = 0, sb = 0, sb3 = 0;
    const rsrc_t rF = mkrsrc(flags, 1u << 20);
    const unsigned evmask = 1u | (1u << (tcs + (wm >> 1))) | (wid < 4 ? 1u << (tcs + 2) : 0u) | (1u << (tcs + 4)) | (1u << (NKT - 2)) | (wid == 0 ? 1u << (NKT - 3) : 0u);
    for (int t = 0; t < NKT; ++t) {
        const int tn = min(t + 2, NKT - 1); constexpr bool go = true;
        SB0();
        { const int slb = (sb3 == 0) ? 2 : sb3 - 1;
          SUBSTEP(A0, B0, A1, B1, sa, sb, 1, 2, go, tn, slb); }
        if (__builtin_expect((evmask >> t) & 1u, 0)) {
            if (t == tcs + 4) WAIT_BAR(0); else WAIT_BAR(GB);
            if (t == 0) { if (lane < 2) __hip_atomic_store(flags + F_LN + (m0 + 8 * j32 + wid) * 2 + lane, __float_as_uint(lnv), RLX_AGENT);
                publish(flags, F_ST + blockIdx.x, T); }
            if (t == tcs + (wm >> 1)) { if (wm & 1) colsum(B1); else colsum(B0); }
            if (t == tcs + 2 && tid < BN) { const LAS float* pc = (const LAS float*)(lds + TBL + 2048) + tid;
                __hip_atomic_store(flags + F_CS + (m0 >> 8) * NCOL + ncg + tid, __float_as_uint((pc[0] + pc[256]) + (pc[512] + pc[768])), RLX_AGENT); }
            if (t == tcs + 4) publish(flags, F_LT + (m0 >> 8) * 32 + j32, T);
            if (t == NKT - 3 && wid == 0) poll_tags(flags, lane < 32 ? F_LT + (m0 >> 8) * 32 + lane : F_LT + (lane & 7) * 32 + j32, T, 40);
            if (t == NKT - 2) {
                if (lane < BN / 4) bl_lds_dev(rF, lds + TBL + 2048 + wid * 1024, lane * 16, (F_CS + wid * NCOL + ncg) * 4);
                if (wid < 2) bl_lds_dev(rF, lds + TBL + wid * 1024, lane * 16, (F_LN + m0 * 2) * 4 + wid * 1024); }
        } else WAIT_BAR(GB);
        SB0();
        sa ^= 32768; sb3 = (sb3 == 2) ? 0 : sb3 + 1; sb = sb3 * BSLOT;
        SUBSTEP(A1, B1, A0, B0, sa, sb, 0, 1, go, tn, t & 1);
    }
#undef RDONE
#undef SUBSTEP
    WAIT_BAR(0);
    if (tid < BN) { float cs = 0.f;
#pragma unroll
        for (int i = 0; i < 8; ++i) cs += ((const LAS float*)(lds + TBL + 2048))[i * 256 + tid];
        ((LAS float*)(lds + TBL + 10240))[tid] = cs; }
    LGKM_BAR();
#pragma unroll
    for (int i = 0; i < 4; ++i) { const f32x2 st = ((const LAS f32x2*)(lds + TBL))[64 * wm + 16 * i + fr];
#pragma unroll
        for (int j = 0; j < NT; ++j) { const f32x4 cs = *(const LAS f32x4*)(lds + TBL + 10240 + (16 * (NT * wn + j) + 4 * g) * 4); acc[i][j] = (acc[i][j] - cs * st[0]) * st[1]; } }
    const int mrow = m0 + 64 * wm + fr;
    if constexpr (NT == 6) {
        const rsrc_t rsq = mkrsrc(qkv, 3 * S_ * D_ * 2);
#pragma unroll
        for (int b = 0; b < 3; ++b) {
            const int col = n0 + 96 * wn + 32 * b, which = col >> 10, head = (col & 1023) >> 5;
            const float sc = (which == 0) ? 0.17677669529663687f * 1.4426950408889634f : 1.0f;
            const int dst = ((which * H_ + head) * S_) * DH;
#pragma unroll
            for (int i = 0; i < 4; ++i) {
                const f32x4 u = acc[i][2 * b] * sc, v = acc[i][2 * b + 1] * sc;
                unsigned x0 = cvtpk(u[0], u[1]), x1 = cvtpk(u[2], u[3]), y0 = cvtpk(v[0], v[1]), y1 = cvtpk(v[2], v[3]);
                auto r0 = __builtin_amdgcn_permlane16_swap(x0, y0, false, false); auto r1 = __builtin_amdgcn_permlane16_swap(x1, y1, false, false);
                const int tok = mrow + 16 * i; int ch = 2 * (g & 1) + (g >> 1); if (which == 1) ch ^= (tok >> 2) & 3;
                st_wt((u32x4){r0[0], r1[0], r0[1], r1[1]}, rsq, (unsigned)(dst + tok * DH + ch * 8) * 2u);
            }
        }
        WAIT_BAR(0);
    } else {
        const rsrc_t rsa = mkrsrc(ao, S_ * LDAO * 2);
        u32x4 sv[14];
        auto pk = [&](const f32x4& a, const f32x4& b) { const f32x4 z = {0.f, 0.f, 0.f, 0.f}; const f32x4 u = __builtin_elementwise_max(a, z), v = __builtin_elementwise_max(b, z);
            unsigned x0 = cvtpk(u[0], u[1]), x1 = cvtpk(u[2], u[3]), y0 = cvtpk(v[0], v[1]), y1 = cvtpk(v[2], v[3]);
            auto r0 = __builtin_amdgcn_permlane16_swap(x0, y0, false, false); auto r1 = __builtin_amdgcn_permlane16_swap(x1, y1, false, false); return (u32x4){r0[0], r1[0], r0[1], r1[1]}; };
        auto off = [&](int s, int half) -> unsigned {
            const int c0 = n0 + 112 * half;
            if (s < 12) return (unsigned)((mrow + 16 * (s & 3)) * LDAO + c0 + 16 * half + 32 * (s >> 2) + 16 * (g & 1) + 8 * (g >> 1)) * 2u;
            return (unsigned)((mrow + 16 * (2 * (s - 12) + (g & 1))) * LDAO + c0 + (half ? 0 : 96) + 8 * (g >> 1)) * 2u; };
        if (wn == 0) {
#pragma unroll
            for (int b = 0; b < 3; ++b)
#pragma unroll
                for (int i = 0; i < 4; ++i) sv[4 * b + i] = pk(acc[i][2 * b], acc[i][2 * b + 1]);
#pragma unroll
            for (int i = 0; i < 2; ++i) sv[12 + i] = pk(acc[2 * i][6], acc[2 * i + 1][6]);
#pragma unroll
            for (int s = 0; s < 14; ++s) *(LAS u32x4*)(lds + HSTG + (wm * 14 + s) * 1024 + lane * 16) = sv[s];
            LGKM_BAR();
        } else {
#pragma unroll
            for (int b = 0; b < 3; ++b)
#pragma unroll
                for (int i = 0; i < 4; ++i) sv[4 * b + i] = pk(acc[i][2 * b + 1], acc[i][2 * b + 2]);
#pragma unroll
            for (int i = 0; i < 2; ++i) sv[12 + i] = pk(acc[2 * i][0], acc[2 * i + 1][0]);
#pragma unroll
            for (int s = 0; s < 14; ++s) st_wt(sv[s], rsa, off(s, 1));
            LGKM_BAR();
#pragma unroll
            for (int s = 0; s < 14; ++s) sv[s] = *(const LAS u32x4*)(lds + HSTG + (wm * 14 + s) * 1024 + lane * 16);
#pragma unroll
            for (int s = 0; s < 14; ++s) st_wt(sv[s], rsa, off(s, 0));
        }
    }
    return T;
}

struct G2 { const bf16_t *ao_, *w2_, *wo_; unsigned aoff, boff[2]; int rb, cb, kh, k0, nkt;
    __device__ __forceinline__ rsrc_t rA() const { return mkrsrc(ao_ + (size_t)rb * 128 * LDAO + k0 * 64, (unsigned)(128 * LDAO - k0 * 64) * 2u); }
    __device__ __forceinline__ rsrc_t rW2() const { return mkrsrc(w2_ + cb * 128, (unsigned)(DFF * D_ - cb * 128) * 2u); }
    __device__ __forceinline__ rsrc_t rWo() const { return mkrsrc(wo_ + cb * 128, (unsigned)(D_ * D_ - cb * 128) * 2u); } };
__device__ __forceinline__ G2 g2_setup(const bf16_t* ao, const bf16_t* w2, const bf16_t* wo, int rb, int cb, int kh, int split) {
    const int lane = threadIdx.x & 63, wid = __builtin_amdgcn_readfirstlane(threadIdx.x >> 6);
    G2 s; s.kh = kh; s.k0 = kh ? split : 0; s.nkt = kh ? 72 - split : split; s.rb = rb; s.cb = cb; s.ao_ = ao; s.w2_ = w2; s.wo_ = wo;
    s.aoff = (unsigned)((16 * wid + (lane >> 3)) * LDAO + (((lane & 7) ^ ((lane >> 3) & 7)) * 8)) * 2u;
#pragma unroll
    for (int i = 0; i < 2; ++i) { const int k = 8 * wid + 4 * i + (lane >> 4), cp = lane & 15, swz = 2 * ((k & 3) | (((k >> 3) & 1) << 2)); s.boff[i] = (unsigned)(k * D_ + (cp ^ swz) * 8) * 2u; }
    return s;
}
__device__ __forceinline__ int g2_slot(int t) { return (int)((0x10002030u >> (8 * (t & 3))) & 0xffu) * 2048; }
__device__ __forceinline__ void g2_stage(lds_ptr lds, const G2& s, int t, int sl) {
    const int wid = __builtin_amdgcn_readfirstlane(threadIdx.x >> 6), gt = s.k0 + t; const lds_ptr d = lds + g2_slot(sl);
#pragma unroll
    for (int i = 0; i < 2; ++i) bl_lds_dev(s.rA(), d + (16 * wid + 8 * i) * 128, s.aoff, t * 128 + i * (8 * LDAO * 2));
    if (gt < 56) {
#pragma unroll
        for (int i = 0; i < 2; ++i) bl_lds_dev(s.rW2(), d + 16384 + (8 * wid + 4 * i) * 256, s.boff[i], gt * (64 * D_ * 2));
    } else {
#pragma unroll
        for (int i = 0; i < 2; ++i) bl_lds_dev(s.rWo(), d + 16384 + (8 * wid + 4 * i) * 256, s.boff[i], (gt - 56) * (64 * D_ * 2));
    }
}

constexpr int AT_KT = 128, AT_STAGE = 4 * AT_KT * 64, AT_NST = 1024 / AT_KT, AT_Q = 122880, AT_FL = 139264;
__device__ __forceinline__ void attn_body(lds_ptr lds, const bf16_t* qkv, bf16_t* ao, unsigned* flags, const unsigned T, const int head, const int qb, const int hpub, const int hwait, const G2& g2) {
    const int tid = threadIdx.x, lane = tid & 63, wid = __builtin_amdgcn_readfirstlane(tid >> 6), wq = wid & 3, kh = wid >> 2;
    const int r32 = lane & 31, hi = lane >> 5;
    const rsrc_t rs = mkrsrc(qkv, 3 * S_ * D_ * 2);
    const unsigned ssrc = (unsigned)((((1 + (wid & 1)) * H_ + head) * S_ + (wid >> 1) * 1024) * 64);
    auto stage = [&](int st, int slot) {
#pragma unroll
        for (int i = 0; i < 8; ++i) bl_lds_dev(rs, lds + slot * AT_STAGE + (wid & 3) * 8192 + i * 1024, lane * 16, ssrc + st * (AT_KT * 64) + i * 1024); };
    if (wid < 4) {
        poll_tags(flags, F_Q + (((lane >> 3) & 3) * 1024 + head * 32) / 192 * 32 + (lane & 7), T, 24);
#pragma unroll
        for (int i = 0; i < 4; ++i) bl_lds_dev(rs, lds + AT_Q + wid * 4096 + i * 1024, lane * 16, (unsigned)((head * S_ + qb * 256) * 64 + wid * 4096 + i * 1024));
    }
    const int kr[2] = {r32 * 64 + ((hi ^ ((r32 >> 2) & 3)) * 16), r32 * 64 + (((2 + hi) ^ ((r32 >> 2) & 3)) * 16)};
    const int vr = AT_KT * 64 + (4 * hi + ((lane & 15) >> 2)) * 64 + ((lane >> 4) & 1) * 32 + (lane & 3) * 8;
    bf16x8 sel;
    { const short one = ((lane & 15) == ((lane >> 4) & 1)) ? (short)0x3f80 : (short)0;
#pragma unroll
      for (int j = 0; j < 8; ++j) sel[j] = one; }
    bf16x8 qf[2][2];
    float mref[2];
    f32x16 o[2]; f32x4 ls[2];
    for (int attempt = 0; attempt < 2; ++attempt) {
        if (wid < 4) { stage(0, 0); stage(1, 1); asm volatile("s_waitcnt vmcnt(8)" ::: "memory"); WAIT_BAR(8); } else LGKM_BAR();
        if (attempt == 0) {
#pragma unroll
            for (int a = 0; a < 2; ++a)
#pragma unroll
                for (int s = 0; s < 2; ++s) qf[a][s] = lds_b128(lds + AT_Q + (wq * 64 + a * 32 + r32) * 64 + (2 * s + hi) * 16);
            const bf16x8 k0 = lds_b128(lds + kr[0]), k1 = lds_b128(lds + kr[1]);
#pragma unroll
            for (int a = 0; a < 2; ++a) { f32x16 s = {}; s = MFMA32(k0, qf[a][0], s); s = MFMA32(k1, qf[a][1], s);
                float m = s[0];
#pragma unroll
                for (int r = 1; r < 16; ++r) m = fmaxf(m, s[r]);
                auto sw = __builtin_amdgcn_permlane32_swap(__float_as_uint(m), __float_as_uint(m), false, false);
                mref[a] = fmaxf(__uint_as_float(sw[0]), __uint_as_float(sw[1])); }
        }
        f32x16 nm[2];
#pragma unroll
        for (int a = 0; a < 2; ++a) {
#pragma unroll
            for (int r = 0; r < 16; ++r) nm[a][r] = -mref[a];
            o[a] = (f32x16){}; ls[a] = (f32x4){}; }
        bf16x8 kA[2], kB[2]; s16x4 vA[4], vB[4]; f32x16 sa_, sb_; u32x4 pa0, pa1, pb0, pb1;
        { const lds_ptr base = lds + kh * (2 * AT_KT * 64);
          kA[0] = lds_b128(base + kr[0]); kA[1] = lds_b128(base + kr[1]);
          sa_ = MFMA32(kA[0], qf[0][0], nm[0]); sa_ = MFMA32(kA[1], qf[0][1], sa_);
          pb0 = (u32x4){0u, 0u, 0u, 0u}; pb1 = pb0;
#pragma unroll
          for (int i = 0; i < 4; ++i) vB[i] = (s16x4){0, 0, 0, 0}; }
#define EXP2(S, i) S[i] = __builtin_amdgcn_exp2f(S[i])
#define PF(x) __builtin_bit_cast(bf16x8, x)
#define ASTEP(SC, PC0, PC1, SN, KF, QN, NMN, ON, LN_, VF, PP0, PP1, R0, R1, R2, R3, R4, R5) do { \
            SN = MFMA32(KF[0], QN[0], NMN); SB0(); EXP2(SC, 0); EXP2(SC, 1); EXP2(SC, 2); R0; SB0(); \
            SN = MFMA32(KF[1], QN[1], SN); SB0(); EXP2(SC, 3); EXP2(SC, 4); EXP2(SC, 5); PC0[0] = cvtpk(SC[0], SC[1]); R1; SB0(); \
            ON = MFMA32(cat4(VF[0], VF[1]), PF(PP0), ON); SB0(); EXP2(SC, 6); EXP2(SC, 7); EXP2(SC, 8); PC0[1] = cvtpk(SC[2], SC[3]); R2; SB0(); \
            ON = MFMA32(cat4(VF[2], VF[3]), PF(PP1), ON); SB0(); EXP2(SC, 9); EXP2(SC, 10); EXP2(SC, 11); PC0[2] = cvtpk(SC[4], SC[5]); R3; SB0(); \
            LN_ = MFMA16(sel, PF(PP0), LN_); SB0(); EXP2(SC, 12); EXP2(SC, 13); PC0[3] = cvtpk(SC[6], SC[7]); PC1[0] = cvtpk(SC[8], SC[9]); R4; SB0(); \
            LN_ = MFMA16(sel, PF(PP1), LN_); SB0(); EXP2(SC, 14); EXP2(SC, 15); PC1[1] = cvtpk(SC[10], SC[11]); PC1[2] = cvtpk(SC[12], SC[13]); R5; PC1[3] = cvtpk(SC[14], SC[15]); asm volatile("" : "+v"(PC0), "+v"(PC1)); SB0(); } while (0)
#define ATILE(KC, VP, KN, VN, kb, vb) do { \
            ASTEP(sa_, pa0, pa1, sb_, KC, qf[1], nm[1], o[1], ls[1], VP, pb0, pb1, KN[0] = lds_b128((kb) + kr[0]), KN[1] = lds_b128((kb) + kr[1]), \
                  VN[0] = lds_tr((vb) + vr), VN[1] = lds_tr((vb) + vr + 512), VN[2] = lds_tr((vb) + vr + 1024), VN[3] = lds_tr((vb) + vr + 1536)); \
            ASTEP(sb_, pb0, pb1, sa_, KN, qf[0], nm[0], o[0], ls[0], VN, pa0, pa1, (void)0, (void)0, (void)0, (void)0, (void)0, (void)0); } while (0)
        const unsigned aev = wid >= 4 ? 0x54u : (0xc0u | (wid == 0 ? 0x24u : 0u) | ((wid == 1 && blockIdx.x == 0) ? 0x10u : 0u));
        int slot = 0;
        for (int st = 0; st < AT_NST; ++st) {
            const int nslot = (slot == 2) ? 0 : slot + 1;
            const lds_ptr base = lds + slot * AT_STAGE + kh * (2 * AT_KT * 64), nbase = lds + nslot * AT_STAGE + kh * (2 * AT_KT * 64);
            SB0();
            ATILE(kA, vB, kB, vA, base + 2048, base);
            ATILE(kB, vA, kA, vB, base + 4096, base + 2048);
            ATILE(kA, vB, kB, vA, base + 6144, base + 4096);
            if (__builtin_expect((aev >> st) & 1u, 0)) {
                if (wid < 4) {
                    if (attempt == 0 && st == 5 && wid == 0) poll_tags(flags, hwait + lane, T, 16);
                    if (attempt == 0 && st == 4) {
#pragma unroll 1
                        for (int i = 0; i < 4; ++i) poll_tags(flags, F_ST + 64 * i + lane, T, 64);
                        if (lane == 0) __hip_atomic_store(flags + F_E, T, RLX_AGENT); }
                    WAIT_BAR(0);
                    if (st + 2 < AT_NST) stage(st + 2, (slot == 0) ? 2 : slot - 1);
                } else {
                    WAIT_BAR(0);
                    if (attempt == 0 && st < 6) {
                        const int w = (blockIdx.x >> 3) * 4 + wid - 4; const bool second = st == 4;
                        const int n = g2.kh ? (second ? 8 : 10) : 9, k0 = g2.kh ? (second ? w * 8 : 2304 + w * 10) : w * 18 + (second ? 9 : 0);
                        const rsrc_t rp = mkrsrc(((g2.kh && second) ? g2.wo_ : g2.w2_) + (g2.cb >> 2) * 512, (DFF * D_ - 512) * 2);
                        for (int i = 0; i < n; ++i) bl_lds(rp, lds + JUNK, lane * 16, (k0 + i) * (D_ * 2)); } }
                if (attempt == 0 && st == 2 && hpub >= 0) publish(flags, hpub, T);
                if (attempt == 0 && st == AT_NST - 2) g2_stage(lds, g2, 0, 0);
            } else if (wid < 4) { WAIT_BAR(0); stage(st + 2, (slot == 0) ? 2 : slot - 1); }
            else LGKM_BAR();
            SB0();
            ATILE(kB, vA, kA, vB, nbase, base + 6144);
            slot = nslot;
        }
        o[1] = MFMA32(cat4(vB[0], vB[1]), PF(pb0), o[1]); o[1] = MFMA32(cat4(vB[2], vB[3]), PF(pb1), o[1]);
        ls[1] = MFMA16(sel, PF(pb0), ls[1]); ls[1] = MFMA16(sel, PF(pb1), ls[1]);
#undef ATILE
#undef ASTEP
#undef EXP2
#undef PF
        LGKM_BAR();
        if (attempt == 1) break;
        bool bad = false;
#pragma unroll
        for (int a = 0; a < 2; ++a) { bad |= !(ls[a][0] < 1.0e18f) || !(ls[a][1] < 1.0e18f);
#pragma unroll
            for (int r = 0; r < 16; ++r) bad |= !(fabsf(o[a][r]) < 1.0e30f); }
        LAS unsigned* fl = (LAS unsigned*)(lds + AT_FL);
        if (lane == 0) fl[wid] = __any(bad) ? 1u : 0u;
        LGKM_BAR();
        const u32x4 f0 = *(const LAS u32x4*)fl, f1 = *(const LAS u32x4*)(fl + 4);
        if ((f0[0] | f0[1] | f0[2] | f0[3] | f1[0] | f1[1] | f1[2] | f1[3]) == 0u) break;
        float mx[2] = {-INFINITY, -INFINITY};
        for (int st = 0; st < AT_NST; ++st) {
            if (wid < 4) stage(st, 0);
            WAIT_BAR(0);
#pragma unroll
            for (int h2 = 0; h2 < 2; ++h2)
                for (int kt = 0; kt < AT_KT / 32; ++kt) {
                    const lds_ptr base = lds + h2 * (2 * AT_KT * 64) + kt * 2048;
                    const bf16x8 k0 = lds_b128(base + kr[0]), k1 = lds_b128(base + kr[1]);
#pragma unroll
                    for (int a = 0; a < 2; ++a) { f32x16 s = {}; s = MFMA32(k0, qf[a][0], s); s = MFMA32(k1, qf[a][1], s);
#pragma unroll
                        for (int r = 0; r < 16; ++r) mx[a] = fmaxf(mx[a], s[r]); }
                }
            LGKM_BAR();
        }
#pragma unroll
        for (int a = 0; a < 2; ++a) { auto sw = __builtin_amdgcn_permlane32_swap(__float_as_uint(mx[a]), __float_as_uint(mx[a]), false, false);
            mref[a] = fmaxf(__uint_as_float(sw[0]), __uint_as_float(sw[1])); }
    }
    g2_stage(lds, g2, 1, 1);
    LAS float* xo = (LAS float*)lds + wq * (2 * 17 * 64);
    if (kh == 1) {
#pragma unroll
        for (int a = 0; a < 2; ++a) {
#pragma unroll
            for (int r = 0; r < 16; ++r) xo[(a * 17 + r) * 64 + lane] = o[a][r];
            if (lane < 16) { xo[(a * 17 + 16) * 64 + lane] = ls[a][0]; xo[(a * 17 + 16) * 64 + 16 + lane] = ls[a][1]; } }
    } else {
#pragma unroll
        for (int a = 0; a < 2; ++a) if (lane < 16) { xo[(a * 17 + 16) * 64 + 32 + lane] = ls[a][0]; xo[(a * 17 + 16) * 64 + 48 + lane] = ls[a][1]; }
    }
    LGKM_BAR();
    if (kh == 0) {
        const rsrc_t rsa = mkrsrc(ao, S_ * LDAO * 2);
#pragma unroll
        for (int a = 0; a < 2; ++a) {
            const float l = xo[(a * 17 + 16) * 64 + r32] + xo[(a * 17 + 16) * 64 + 32 + r32];
            const float rl = 1.0f / l;
            unsigned pk[8];
#pragma unroll
            for (int r = 0; r < 8; ++r) pk[r] = cvtpk((o[a][2 * r] + xo[(a * 17 + 2 * r) * 64 + lane]) * rl, (o[a][2 * r + 1] + xo[(a * 17 + 2 * r + 1) * 64 + lane]) * rl);
            const int dst = (qb * 256 + wq * 64 + a * 32 + r32) * LDAO + DFF + head * DH;
#pragma unroll
            for (int k = 0; k < 4; k += 2) {
                auto r0 = __builtin_amdgcn_permlane32_swap(pk[2 * k], pk[2 * k + 2], false, false); auto r1 = __builtin_amdgcn_permlane32_swap(pk[2 * k + 1], pk[2 * k + 3], false, false);
                st_wt((u32x4){r0[0], r1[0], r0[1], r1[1]}, rsa, (unsigned)(dst + 8 * k + 8 * hi) * 2u);
            }
        }
    }
    LGKM_BAR();
}

__device__ __forceinline__ void gemm2_body(lds_ptr lds, const G2& g2, const bf16_t* __restrict__ x, bf16_t* __restrict__ out, float* part, unsigned* flags, const unsigned T, const int rb, const int cb, const int kh, const int opub) {
    const int NKT = g2.nkt;
    const int tid = threadIdx.x, lane = tid & 63, wid = __builtin_amdgcn_readfirstlane(tid >> 6), kg = wid >> 2, wm = (wid >> 1) & 1, wn = wid & 1;
    const int fr = lane & 15, g = lane >> 4, q = fr >> 2, p = lane & 3;
    const int m0 = rb * 128, n0 = cb * 128;
    const int ar = (64 * wm + fr) * 128 + (((4 * kg + g) ^ (fr & 7)) * 16);
    int br[4];
    { const int swz = 2 * (q | ((g & 1) << 2));
#pragma unroll
      for (int j = 0; j < 4; ++j) br[j] = 16384 + (32 * kg + 8 * g + q) * 256 + (((2 * (4 * wn + j) + (p >> 1)) ^ swz) * 16) + (p & 1) * 8; }
    f32x4 acc[4][4];
#pragma unroll
    for (int i = 0; i < 4; ++i)
#pragma unroll
        for (int j = 0; j < 4; ++j) acc[i][j] = (f32x4){0.f, 0.f, 0.f, 0.f};
    bf16x8 A0[4], A1[4]; s16x4 B0[4][2], B1[4][2];
    const int wid4 = wid; const rsrc_t rA2 = g2.rA(), rW2 = g2.rW2(), rWo = g2.rWo();
    auto st1 = [&](int t, int sl, int i) {
        const int gt = g2.k0 + t; const lds_ptr d = lds + g2_slot(sl);
        if (i < 2) dma16<true, 0>(rA2, d + (16 * wid4 + 8 * i) * 128, g2.aoff, t * 128 + i * (8 * LDAO * 2));
        else { const bool w = gt < 56; dma16<true, 0>(w ? rW2 : rWo, d + 16384 + (8 * wid4 + 4 * (i - 2)) * 256, g2.boff[i - 2], (w ? gt : gt - 56) * (64 * D_ * 2)); } };
#define RDONE(AN, BN_, so, r) do { if ((r) < 4) AN[(r) & 3] = lds_b128(lds + (so) + ar + ((r) & 3) * 2048); \
        else if ((r) < 12) BN_[(((r) - 4) >> 1) & 3][(r) & 1] = lds_tr(lds + (so) + br[(((r) - 4) >> 1) & 3] + ((r) & 1) * 1024); } while (0)
#define STEP(AC, BC, AN, BN_, so, tl, sl) do { \
        unroll<0, 16>([&](auto ic) { constexpr int idx = decltype(ic)::value; constexpr int j = idx >> 2, i = idx & 3; \
            acc[i][j] = MFMA16(cat4(BC[j][0], BC[j][1]), AC[i], acc[i][j]); SB0(); \
            RDONE(AN, BN_, so, idx); \
            if ((idx & 3) == 2) st1(tl, sl, idx >> 2); \
            SB0(); }); } while (0)
    g2_stage(lds, g2, 2, 2); g2_stage(lds, g2, 3, 3);
    WAIT_BAR(8);
    unroll<0, 12>([&](auto ic) { constexpr int idx = decltype(ic)::value; RDONE(A0, B0, g2_slot(0), idx); });
    WAIT_BAR(8);
    for (int t = 0; t < NKT; t += 2) {
        if (kh == 1 && t == 48 - g2.k0 && wid == 0) poll_tags(flags, F_O + (rb >> 1) * 32 + (lane & 31), T, 32);
        SB0();
        STEP(A0, B0, A1, B1, g2_slot(t + 1), min(t + 4, NKT - 1), t);
        WAIT_BAR(8); SB0();
        STEP(A1, B1, A0, B0, g2_slot(t + 2), min(t + 5, NKT - 1), t + 1);
        if (__builtin_expect(t == 2, 0)) { if (wid < 4) WAIT_BAR(0); else WAIT_BAR(8); publish(flags, opub, T); }
        else WAIT_BAR(8);
    }
#undef RDONE
#undef STEP
    WAIT_BAR(0);
    const int mrow = m0 + 64 * wm + 32 * kg + 16 * kh + fr;
    u32x2 xv[4];
#pragma unroll
    for (int j = 0; j < 4; ++j) xv[j] = *(const u32x2*)(x + (size_t)mrow * D_ + n0 + 64 * wn + 16 * j + 4 * g);
    LAS f32x4* xr = (LAS f32x4*)lds + (wid & 3) * 1024;
#pragma unroll
    for (int i = 0; i < 2; ++i)
#pragma unroll
        for (int j = 0; j < 4; ++j) xr[(kg * 8 + i * 4 + j) * 64 + lane] = kg ? acc[i][j] : acc[2 + i][j];
    LGKM_BAR();
    f32x4 r[2][4];
#pragma unroll
    for (int i = 0; i < 2; ++i)
#pragma unroll
        for (int j = 0; j < 4; ++j) r[i][j] = (kg ? acc[2 + i][j] : acc[i][j]) + xr[((kg ^ 1) * 8 + i * 4 + j) * 64 + lane];
    const int tile = rb * 8 + cb;
    { const rsrc_t rs = mkrsrc(part + (size_t)(tile * 2 + kh) * 8192, 32768);
#pragma unroll
      for (int j = 0; j < 4; ++j) st_wt(__builtin_bit_cast(u32x4, kh ? r[0][j] : r[1][j]), rs, (wid * 4 + j) * 1024 + lane * 16); }
    asm volatile("s_waitcnt vmcnt(0)" ::: "memory");
    if (lane == 0) __hip_atomic_store(flags + F_P + (tile * 2 + kh) * 32 + wid, T, RLX_AGENT);
    poll_tags(flags, F_P + (tile * 2 + (kh ^ 1)) * 32 + wid, T, 1);
    { const rsrc_t rs = mkrsrc(part + (size_t)(tile * 2 + (kh ^ 1)) * 8192, 32768);
      f32x4 o[4];
#pragma unroll
      for (int j = 0; j < 4; ++j) o[j] = __builtin_bit_cast(f32x4, __builtin_amdgcn_raw_buffer_load_b128(rs, (wid * 4 + j) * 1024 + lane * 16, 0, 16));
      unsigned pk[4][2];
#pragma unroll
      for (int j = 0; j < 4; ++j) { f32x4 v = (kh ? r[1][j] : r[0][j]) + o[j];
          v[0] += __uint_as_float(xv[j][0] << 16); v[1] += __uint_as_float(xv[j][0] & 0xffff0000u); v[2] += __uint_as_float(xv[j][1] << 16); v[3] += __uint_as_float(xv[j][1] & 0xffff0000u);
          pk[j][0] = cvtpk(v[0], v[1]); pk[j][1] = cvtpk(v[2], v[3]); }
#pragma unroll
      for (int j = 0; j < 4; j += 2) {
          auto r0 = __builtin_amdgcn_permlane16_swap(pk[j][0], pk[j + 1][0], false, false); auto r1 = __builtin_amdgcn_permlane16_swap(pk[j][1], pk[j + 1][1], false, false);
          *(u32x4*)(out + (size_t)mrow * D_ + n0 + 64 * wn + 16 * (j + (g & 1)) + 8 * (g >> 1)) = (u32x4){r0[0], r1[0], r0[1], r1[1]}; } }
}

__global__ __launch_bounds__(512, 2) void fused_kernel(const bf16_t* __restrict__ wqkv, const bf16_t* __restrict__ w1, const bf16_t* __restrict__ w2, const bf16_t* __restrict__ wo,
                                                       const bf16_t* __restrict__ x, bf16_t* qkv, bf16_t* ao, bf16_t* __restrict__ out, float* part, unsigned* flags) {
    __shared__ __attribute__((aligned(16))) char smem[LDS_BYTES];
    const lds_ptr lds = (lds_ptr)smem;
    const int xg = blockIdx.x & 7, r = blockIdx.x >> 3;
    {
        unsigned long long pc; unsigned len;
        asm volatile("s_getpc_b64 %0\n.Lfk_beg:\n\ts_mov_b32 %1, .Lfk_end-.Lfk_beg" : "=s"(pc), "=s"(len));
        if (threadIdx.x >= 448) { const rsrc_t rc = mkrsrc((const void*)pc, len);
            bl_lds(rc, lds + JUNK, (threadIdx.x & 63) * 16, r * 2048); bl_lds(rc, lds + JUNK, (threadIdx.x & 63) * 16, r * 2048 + 1024); }
    }
    const int rb1 = (xg & 1) * 4 + (r & 3), c1 = (xg >> 1) * 4 + ((r >> 2) & 3), ffn = r >> 4;
    unsigned T;
    if (ffn) T = gemm1_body<7>(lds, x, w1, DFF, rb1 * 256, c1 * 224, qkv, ao, flags, 16 + c1, wqkv, w1);
    else { T = gemm1_body<6>(lds, x, wqkv, 3 * D_, rb1 * 256, c1 * 192, qkv, ao, flags, c1, wqkv, w1); publish(flags, F_Q + c1 * 32 + rb1, T); }
    const int kh = xg & 1, rr = kh ? (r ^ 16) : r, rb3 = ((xg >> 1) & 1) * 8 + (rr & 7), cb3 = (xg >> 2) * 4 + (rr >> 3);
    const G2 g2 = g2_setup(ao, w2, wo, rb3, cb3, kh, (rr >> 4) ? 72 - SPLITQ : SPLITQ);
    const int head = xg * 4 + (r >> 3), qb = r & 7;
    attn_body(lds, qkv, ao, flags, T, head, qb, ffn ? F_H + rb1 * 32 + c1 : -1, F_H + (rb3 >> 1) * 32, g2);
    gemm2_body(lds, g2, x, out, part, flags, T, rb3, cb3, kh, F_O + qb * 32 + head);
    asm volatile(".Lfk_end:");
}

extern "C" void kernel_launch(void* const* d_in, const int* in_sizes, int n_in, void* d_out, int out_size, void* d_ws, size_t ws_size, hipStream_t stream) {
    const bf16_t* x = (const bf16_t*)d_in[0]; const bf16_t* wqkv = (const bf16_t*)d_in[1]; const bf16_t* wo = (const bf16_t*)d_in[2];
    const bf16_t* w1 = (const bf16_t*)d_in[4]; const bf16_t* w2 = (const bf16_t*)d_in[5];
    char* ws = (char*)d_ws;
    unsigned* flags = (unsigned*)(ws + WS_FLAGS); bf16_t* qkv = (bf16_t*)(ws + WS_QKV); bf16_t* ao = (bf16_t*)(ws + WS_AO); float* part = (float*)(ws + WS_PART);
    fused_kernel<<<256, 512, 0, stream>>>(wqkv, w1, w2, wo, x, qkv, ao, (bf16_t*)d_out, part, flags);
}
```
